```python
import torch

def _softmax(x, axis=-1):
    return torch.softmax(x, dim=axis)
def _gelu(x):
    return torch.nn.functional.gelu(x, approximate="tanh")
def _silu(x):
    return torch.nn.functional.silu(x)
def _sigmoid(x):
    return torch.sigmoid(x)
def _layernorm(x, axis=-1, eps=1e-5):
    m = x.mean(dim=axis, keepdim=True)
    v = x.var(dim=axis, keepdim=True, unbiased=False)
    return (x - m) / torch.sqrt(v + eps)
def _scatter_add(tgt, idx, src, axis=0):
    return tgt.clone().index_add_(axis, idx, src)
def _segment_sum(src, seg, n):
    out = torch.zeros((n,) + src.shape[1:], dtype=src.dtype)
    return out.index_add_(0, seg, src)
def _pad(x, pw):
    flat = tuple(v for pair in reversed(pw) for v in pair)
    return torch.nn.functional.pad(x, flat)

kernel_name = 'sequential_preLN__spectral_gfnet__mixer_token_mlp__prompt'

def setup_inputs():

    _w = (lambda a: (lambda t: t.to(torch.bfloat16) if t.is_floating_point() else t)(torch.as_tensor(a)))
    def _A_setup():
        B, S, D = 1, 2048, 448
        Df = D // 2 + 1
        return {
            "x": _w((lambda *s: torch.randn(*s, dtype=torch.bfloat16) * 0.05)(B, S, D)),
            "w_r": _w((lambda *s: torch.randn(*s, dtype=torch.bfloat16) * 0.4)(S, Df) * 0.02),
            "w_i": _w((lambda *s: torch.randn(*s, dtype=torch.bfloat16) * 0.4)(S, Df) * 0.02),
        }
    _A_in = _A_setup()

    def _B_setup():
        B, S, D, F = 1, 2048, 448, 1792
        return {
            "x": _w((lambda *s: torch.randn(*s, dtype=torch.bfloat16) * 0.05)(B, S, D)),
            "w1": _w((lambda *s: torch.randn(*s, dtype=torch.bfloat16) * 0.4)(S, F) * (S ** -0.5)),
            "b1": _w((lambda *s: torch.randn(*s, dtype=torch.bfloat16) * 3.2)(F) * 0.02),
            "w2": _w((lambda *s: torch.randn(*s, dtype=torch.bfloat16) * 0.2)(F, S) * (F ** -0.5)),
            "b2": _w((lambda *s: torch.randn(*s, dtype=torch.bfloat16) * 0.4)(S) * 0.02),
        }
    _B_in = _B_setup()

    return {
        "A_x": _A_in["x"],
        "A_w_r": _A_in["w_r"],
        "A_w_i": _A_in["w_i"],
        "B_x": _B_in["x"],
        "B_w1": _B_in["w1"],
        "B_b1": _B_in["b1"],
        "B_w2": _B_in["w2"],
        "B_b2": _B_in["b2"],
    }

def reference(A_x, A_w_r, A_w_i, B_x, B_w1, B_b1, B_w2, B_b2):
    def _A_ref(x, w_r, w_i):
        B, S, D = x.shape
        X = (lambda x, axes=(-2,-1): torch.fft.rfft2(x.float(), dim=axes))(x, axes=(-2, -1))
        W = w_r + 1j * w_i
        X = X * W
        y = (lambda x, s, axes=(-2,-1): torch.fft.irfft2(x, s=s, dim=axes).to(torch.bfloat16))(X, (S, D), axes=(-2, -1))
        return y

    def _B_ref(x, w1, b1, w2, b2):
        B, S, D = x.shape
        h = _layernorm(x, axis=-1)
        u = torch.einsum("bsd,sf->bdf", h, w1) + b1
        u = _gelu(u)
        y = torch.einsum("bdf,fs->bds", u, w2) + b2
        return (lambda x, *d: x.permute(*d))(y, 0, 2, 1)

    x = A_x
    h = x + _A_ref(_layernorm(x, axis=-1), A_w_r, A_w_i)
    h2 = h + _B_ref(_layernorm(h, axis=-1), B_w1, B_b1, B_w2, B_b2)
    return h2
```

```cpp
#include <hip/hip_runtime.h>
#include <stdint.h>

typedef _Float16 h8 __attribute__((ext_vector_type(8)));
typedef _Float16 h2 __attribute__((ext_vector_type(2)));
typedef __bf16 b8 __attribute__((ext_vector_type(8)));
typedef __bf16 b2 __attribute__((ext_vector_type(2)));
typedef short s4 __attribute__((ext_vector_type(4)));
typedef short s8 __attribute__((ext_vector_type(8)));
typedef float f2 __attribute__((ext_vector_type(2)));
typedef float f4 __attribute__((ext_vector_type(4)));
typedef float f16v __attribute__((ext_vector_type(16)));
typedef unsigned u2 __attribute__((ext_vector_type(2)));
typedef unsigned u4 __attribute__((ext_vector_type(4)));
#define DI __device__ __forceinline__
#define LDSP __attribute__((address_space(3)))

constexpr int S_ = 2048, D_ = 448, DF_ = 225, F_ = 1792;

namespace tb {
constexpr double PI = 3.14159265358979323846264338327950288;
constexpr double cos_rev(int num, int den) {
  num %= den; if (num < 0) num += den;
  if (2 * num > den) num = den - num;
  if (4 * num == den) return 0.0;
  double sg = 1.0, a = 2.0 * PI * num / den;
  if (4 * num > den) { sg = -1.0; a = PI * (den - 2 * num) / den; }
  double t = 1.0, s = 1.0, a2 = a * a;
  for (int i = 1; i <= 13; ++i) { t = -t * a2 / ((2 * i - 1) * (2 * i)); s += t; }
  return sg * s;
}
constexpr unsigned short f2h(double v) {
  if (v == 0.0) return 0;
  unsigned sg = v < 0 ? 0x8000u : 0u; if (v < 0) v = -v;
  int e = 0; double p = 1.0;
  while (v >= 2.0 * p) { p *= 2.0; ++e; }
  while (v < p) { p *= 0.5; --e; }
  if (e < -14) { e = -14; p = 1.0 / 16384.0; }
  double q = v / p * 1024.0;
  long qi = (long)q; double fr = q - (double)qi;
  if (fr > 0.5 || (fr == 0.5 && (qi & 1))) ++qi;
  long bits = qi < 1024 ? qi : (long)((e + 15) << 10) + (qi - 1024);
  return (unsigned short)(sg | (unsigned)bits);
}
struct Base { unsigned short c1[448], cq[448], c2[448], c8[64]; };
constexpr Base make_base() {
  Base b{};
  for (int i = 0; i < 448; ++i) { double c = cos_rev(i, 448); b.c1[i] = f2h(c); b.cq[i] = f2h(0.25 * c); b.c2[i] = f2h(2.0 * c); }
  for (int i = 0; i < 64; ++i) b.c8[i] = f2h(0.125 * cos_rev(i, 64));
  return b;
}
constexpr Base BASE = make_base();
constexpr int m448(int m) { return ((m % 448) + 448) % 448; }
constexpr unsigned short neg(unsigned short v) { return (unsigned short)(v ^ 0x8000u); }
struct T1 { unsigned short v[8][64][8]; };
constexpr T1 make_t1() {
  T1 t{};
  for (int l = 0; l < 64; ++l) for (int j = 0; j < 8; ++j) {
    const int n = l & 15, g = l >> 4;
    {
      const int r = n >> 3, ka = n & 7, rp = g >> 1, d1 = 7 * (g & 1) + j;
      const bool on = (r == rp) && j < 7;
      t.v[0][l][j] = on ? BASE.c1[m448(32 * d1 * ka)] : 0;
      t.v[1][l][j] = on ? neg(BASE.c1[m448(32 * d1 * ka - 112)]) : 0;
    }
    for (int tt = 0; tt < 2; ++tt) {
      const int kb = 16 * tt + n, d2 = 8 * g + j;
      const unsigned short c = BASE.cq[m448(14 * d2 * kb)], s = BASE.cq[m448(14 * d2 * kb - 112)];
      t.v[2 + 3 * tt][l][j] = c; t.v[3 + 3 * tt][l][j] = s; t.v[4 + 3 * tt][l][j] = tt ? neg(c) : neg(s);
    }
  }
  return t;
}
struct T2 { unsigned short a[4][4][64][8]; };
constexpr T2 make_t2() {
  T2 t{};
  for (int w = 0; w < 4; ++w) for (int ks = 0; ks < 4; ++ks) for (int l = 0; l < 64; ++l) for (int jj = 0; jj < 4; ++jj) {
    const int k1a = 16 * w + (l & 15), s1 = 16 * ks + 4 * (l >> 4) + jj, m = s1 * k1a;
    t.a[w][ks][l][2 * jj] = BASE.c8[m & 63];
    t.a[w][ks][l][2 * jj + 1] = BASE.c8[(m - 16) & 63];
  }
  return t;
}
struct T2b { unsigned short b[2][3][64][8]; };
constexpr T2b make_t2b() {
  T2b t{};
  for (int tt = 0; tt < 2; ++tt) for (int l = 0; l < 64; ++l) for (int j = 0; j < 8; ++j) {
    const int kk = 16 * tt + (l & 15), ss = 16 * (j >> 2) + 4 * (l >> 4) + (j & 3);
    const unsigned short c = BASE.cq[m448(14 * kk * ss)], s = BASE.cq[m448(14 * kk * ss - 112)];
    t.b[tt][0][l][j] = c; t.b[tt][1][l][j] = s; t.b[tt][2][l][j] = neg(s);
  }
  return t;
}
struct T3 { unsigned short v[10][64][8]; };
constexpr T3 make_t3() {
  T3 t{};
  for (int l = 0; l < 64; ++l) for (int j = 0; j < 8; ++j) {
    const int n = l & 15, g = l >> 4;
    for (int tt = 0; tt < 2; ++tt) for (int ks = 0; ks < 2; ++ks) {
      const int kb = 16 * ks + 4 * g + (j >> 1), part = j & 1, d2 = 2 * n + tt;
      const unsigned short c = BASE.cq[m448(14 * d2 * kb)], s = BASE.cq[m448(14 * d2 * kb - 112)];
      t.v[(tt * 2 + 0) * 2 + ks][l][j] = part == 0 ? c : (ks == 0 ? s : neg(s));
      t.v[(tt * 2 + 1) * 2 + ks][l][j] = part == 0 ? s : (ks == 0 ? neg(c) : c);
    }
    for (int u = 0; u < 2; ++u) {
      const int gp = n >> 2, rg = n & 3, e = 4 * u + rg, d1 = 7 * (gp & 1) + e;
      const int ka = 4 * (g & 1) + (j & 3), part = j >> 2;
      const bool on = ((gp >> 1) == (g >> 1)) && e < 7;
      const bool one = (ka == 0 || ka == 7);
      const int idx = m448(32 * d1 * ka - (part ? 112 : 0));
      unsigned short val = one ? BASE.c1[idx] : BASE.c2[idx];
      if (part) val = neg(val);
      t.v[8 + u][l][j] = on ? val : 0;
    }
  }
  return t;
}
}
__device__ __attribute__((aligned(16))) const tb::T1 TAB1 = tb::make_t1();
__device__ __attribute__((aligned(16))) const tb::T2 TAB2 = tb::make_t2();
__device__ __attribute__((aligned(16))) const tb::T2b TAB2B = tb::make_t2b();
__device__ __attribute__((aligned(16))) const tb::T3 TAB3 = tb::make_t3();

DI float bfr(float x) { return (float)(__bf16)x; }
DI float bflo(unsigned v) { return __uint_as_float(v << 16); }
DI float bfhi(unsigned v) { return __uint_as_float(v & 0xffff0000u); }
DI unsigned pk_bf(float a, float b) { f2 v = {a, b}; return __builtin_bit_cast(unsigned, __builtin_convertvector(v, b2)); }
DI unsigned pk_h(float a, float b) { f2 v = {a, b}; return __builtin_bit_cast(unsigned, __builtin_convertvector(v, h2)); }
DI h8 mk_h8(unsigned a, unsigned b, unsigned c, unsigned d) { u4 v = {a, b, c, d}; return __builtin_bit_cast(h8, v); }
template <int C> DI float dpp_add(float v) {
  return v + __uint_as_float((unsigned)__builtin_amdgcn_update_dpp(0, (int)__float_as_uint(v), C, 0xf, 0xf, false));
}
DI float sum32(float v) {
  v = dpp_add<0x128>(v); v = dpp_add<0x124>(v); v = dpp_add<0x122>(v); v = dpp_add<0x121>(v);
  auto r = __builtin_amdgcn_permlane16_swap(__float_as_uint(v), __float_as_uint(v), false, false);
  return __uint_as_float(r[0]) + __uint_as_float(r[1]);
}
#define MFMA16H(a, b, c) __builtin_amdgcn_mfma_f32_16x16x32_f16((a), (b), (c), 0, 0, 0)
DI h8 ldfrag(const unsigned short* p, int lane) { return *(const h8*)(p + lane * 8); }
DI void glds16(const void* g, void* l) {
  __builtin_amdgcn_global_load_lds((const __attribute__((address_space(1))) void*)g, (LDSP void*)l, 16, 0, 0);
}

__global__ __launch_bounds__(512) void k1_rowfft(const unsigned* __restrict__ x, const u4* __restrict__ wr,
                                                 const u4* __restrict__ wi, u4* __restrict__ X1T, u4* __restrict__ WT) {
  __shared__ __attribute__((aligned(16))) unsigned lds[2 * 928 + 2 * 904];
  const int b = blockIdx.x, tid = threadIdx.x;
  const int q = (b & 7) * 32 + (b >> 3);
  if (tid < 256) {
    const int wv = tid >> 6, lane = tid & 63, n = lane & 15, g = lane >> 4;
    const int s = 32 * (8 * (q & 7) + 2 * wv + (g >> 1)) + (q >> 3);
    const unsigned* xp = x + s * 224 + (g & 1) * 112 + n;
    unsigned xv[7];
#pragma unroll
    for (int j = 0; j < 7; ++j) xv[j] = xp[16 * j];
    h8 tb1[8];
#pragma unroll
    for (int i = 0; i < 8; ++i) tb1[i] = ldfrag(&TAB1.v[i][0][0], lane);
    const int ka = n & 7;
    float tc[8], ts[8];
#pragma unroll
    for (int i = 0; i < 8; ++i) {
      const float a = (float)((8 * g + i) * ka) * (1.0f / 448.0f);
      tc[i] = 0.25f * __builtin_amdgcn_cosf(a); ts[i] = 0.25f * __builtin_amdgcn_sinf(a);
    }
    float v[14], sm = 0.f;
#pragma unroll
    for (int j = 0; j < 7; ++j) { v[2 * j] = bflo(xv[j]); v[2 * j + 1] = bfhi(xv[j]); sm += v[2 * j] + v[2 * j + 1]; }
    const float mean = sum32(sm) * (1.0f / 448.0f);
    float sq = 0.f;
#pragma unroll
    for (int j = 0; j < 14; ++j) { const float d = v[j] - mean; sq += d * d; }
    const float var = sum32(sq) * (1.0f / 448.0f);
    const float rs = 1.0f / bfr(sqrtf(bfr(bfr(var) + 1e-5f)));
    const float mb = bfr(mean);
#pragma unroll
    for (int j = 0; j < 14; ++j) v[j] = (v[j] - mb) * rs;
    const f4 z = {0.f, 0.f, 0.f, 0.f};
    f4 tre[2], tim[2];
#pragma unroll
    for (int t = 0; t < 2; ++t) {
      const h8 a = mk_h8(pk_h(v[t], v[2 + t]), pk_h(v[4 + t], v[6 + t]), pk_h(v[8 + t], v[10 + t]), pk_h(v[12 + t], 0.f));
      tre[t] = MFMA16H(a, tb1[0], z); tim[t] = MFMA16H(a, tb1[1], z);
    }
    float pr[8], pi[8];
#pragma unroll
    for (int i = 0; i < 8; ++i) {
      const float a = tre[i & 1][i >> 1], bb = tim[i & 1][i >> 1];
      pr[i] = a * tc[i] + bb * ts[i]; pi[i] = bb * tc[i] - a * ts[i];
    }
    const h8 bre = mk_h8(pk_h(pr[0], pr[1]), pk_h(pr[2], pr[3]), pk_h(pr[4], pr[5]), pk_h(pr[6], pr[7]));
    const h8 bim = mk_h8(pk_h(pi[0], pi[1]), pk_h(pi[2], pi[3]), pk_h(pi[4], pi[5]), pk_h(pi[6], pi[7]));
    const f4 xr0 = MFMA16H(tb1[3], bim, MFMA16H(tb1[2], bre, z));
    const f4 xi0 = MFMA16H(tb1[2], bim, MFMA16H(tb1[4], bre, z));
    const f4 xr1 = MFMA16H(tb1[6], bim, MFMA16H(tb1[5], bre, z));
    const f4 xi1 = MFMA16H(tb1[7], bim, MFMA16H(tb1[6], bre, z));
    unsigned* o = lds + (wv >> 1) * 928 + 2 * (wv & 1) + (n >> 3);
    const int k0 = ka + 56 * g, k1 = 224 - k0;
#pragma unroll
    for (int r = 0; r < 4; ++r) o[4 * (k0 + 14 * r)] = pk_h(xr0[r], xi0[r]);
#pragma unroll
    for (int r = 0; r < 4; ++r) o[4 * (k1 - 14 * r)] = pk_h(xr1[r], xi1[r]);
    __syncthreads();
#pragma unroll
    for (int p = 0; p < 2; ++p) {
      const int i = tid + 256 * p;
      if (i < 450) {
        const int k2 = i >> 1, hf = i & 1;
        X1T[k2 * 512 + q * 2 + hf] = *(const u4*)(lds + hf * 928 + k2 * 4);
      }
    }
  } else {
    const int t = tid - 256;
    unsigned* lw = lds + 2 * 928;
    if (t < 225) {
      const u4 a = wr[q * 225 + t], c = wi[q * 225 + t];
      *(u4*)(lw + 4 * t) = a; *(u4*)(lw + 904 + 4 * t) = c;
    }
    __syncthreads();
    const unsigned short* lr = (const unsigned short*)lw;
#pragma unroll
    for (int p = 0; p < 2; ++p) {
      const int i = t + 256 * p;
      if (i < 450) {
        const int k2 = i >> 1, hf = i & 1;
        u4 o;
#pragma unroll
        for (int r = 0; r < 4; ++r) {
          const int e = (4 * hf + r) * 225 + k2;
          o[r] = (unsigned)lr[e] | ((unsigned)lr[1808 + e] << 16);
        }
        WT[k2 * 512 + q * 2 + hf] = o;
      }
    }
  }
}

DI void dft64(const unsigned char* img, int n, int g, const h8 (&bc)[4], const h8 (&bs)[4], f4 (&re)[2], f4 (&im)[2]) {
  const f4 z = {0.f, 0.f, 0.f, 0.f};
#pragma unroll
  for (int t = 0; t < 2; ++t) {
    re[t] = z; im[t] = z;
#pragma unroll
    for (int ks = 0; ks < 4; ++ks) {
      const h8 a = *(const h8*)(img + (16 * t + n) * 256 + (((4 * ks + g) ^ n) << 4));
      re[t] = MFMA16H(a, bc[ks], re[t]); im[t] = MFMA16H(a, bs[ks], im[t]);
    }
  }
}
__global__ __launch_bounds__(256) void k2_colfft(const unsigned char* __restrict__ X1T, const unsigned* __restrict__ WT,
                                                 u4* __restrict__ Y1T) {
  __shared__ __attribute__((aligned(16))) unsigned char lds[16384];
  const int k2 = blockIdx.x, tid = threadIdx.x;
  const int w = __builtin_amdgcn_readfirstlane(tid >> 6), lane = tid & 63, n = lane & 15, g = lane >> 4;
#pragma unroll
  for (int i = 0; i < 2; ++i) {
    const int row = 8 * w + 4 * i + g;
    glds16(X1T + k2 * 8192 + row * 256 + ((n ^ (row & 15)) << 4), lds + (8 * w + 4 * i) * 256);
  }
  unsigned wv[8];
  const unsigned* wp = WT + k2 * 2048 + 256 * g + 16 * w + n;
#pragma unroll
  for (int i = 0; i < 8; ++i) wv[i] = wp[1024 * (i >> 2) + 64 * (i & 3)];
  h8 bc[4], bs[4], fb[6];
#pragma unroll
  for (int i = 0; i < 4; ++i) bc[i] = ldfrag(&TAB2.a[w][i][0][0], lane);
#pragma unroll
  for (int i = 0; i < 6; ++i) fb[i] = ldfrag(&TAB2B.b[0][i][0][0], lane);
  float c1[8], s1[8], c2[8], s2[8];
#pragma unroll
  for (int i = 0; i < 8; ++i) {
    const float a = (float)((16 * (i >> 2) + 4 * g + (i & 3)) * (16 * w + n)) * (1.0f / 2048.0f);
    c1[i] = 16.0f * __builtin_amdgcn_cosf(a); s1[i] = 16.0f * __builtin_amdgcn_sinf(a);
    const float b = (float)((16 * (i >> 2) + n) * (16 * w + 4 * g + (i & 3))) * (1.0f / 2048.0f);
    c2[i] = __builtin_amdgcn_cosf(b); s2[i] = __builtin_amdgcn_sinf(b);
  }
#pragma unroll
  for (int i = 0; i < 4; ++i) {
    u4 u = __builtin_bit_cast(u4, bc[i]);
#pragma unroll
    for (int j = 0; j < 4; ++j) u[j] = __builtin_amdgcn_alignbit(u[j], u[j], 16) ^ 0x8000u;
    bs[i] = __builtin_bit_cast(h8, u);
  }
  asm volatile("s_waitcnt vmcnt(0)" ::: "memory");
  __syncthreads();
  const f4 z = {0.f, 0.f, 0.f, 0.f};
  f4 re[2], im[2];
  dft64(lds, n, g, bc, bs, re, im);
  float pr[8], pi[8];
#pragma unroll
  for (int i = 0; i < 8; ++i) {
    const float a = re[i >> 2][i & 3], b = im[i >> 2][i & 3];
    pr[i] = a * c1[i] + b * s1[i]; pi[i] = b * c1[i] - a * s1[i];
  }
  h8 dr = mk_h8(pk_h(pr[0], pr[1]), pk_h(pr[2], pr[3]), pk_h(pr[4], pr[5]), pk_h(pr[6], pr[7]));
  h8 di = mk_h8(pk_h(pi[0], pi[1]), pk_h(pi[2], pi[3]), pk_h(pi[4], pi[5]), pk_h(pi[6], pi[7]));
#pragma unroll
  for (int t = 0; t < 2; ++t) {
    re[t] = MFMA16H(fb[3 * t + 1], di, MFMA16H(fb[3 * t], dr, z));
    im[t] = MFMA16H(fb[3 * t], di, MFMA16H(fb[3 * t + 2], dr, z));
  }
#pragma unroll
  for (int i = 0; i < 8; ++i) {
    const float a = re[i >> 2][i & 3], b = im[i >> 2][i & 3], wr = bflo(wv[i]), wi = bfhi(wv[i]);
    pr[i] = a * wr - b * wi; pi[i] = a * wi + b * wr;
  }
  dr = mk_h8(pk_h(pr[0], pr[1]), pk_h(pr[2], pr[3]), pk_h(pr[4], pr[5]), pk_h(pr[6], pr[7]));
  di = mk_h8(pk_h(pi[0], pi[1]), pk_h(pi[2], pi[3]), pk_h(pi[4], pi[5]), pk_h(pi[6], pi[7]));
#pragma unroll
  for (int t = 0; t < 2; ++t) {
    re[t] = MFMA16H(di, fb[3 * t + 2], MFMA16H(dr, fb[3 * t], z));
    im[t] = MFMA16H(di, fb[3 * t], MFMA16H(dr, fb[3 * t + 1], z));
  }
  unsigned char* img2 = lds + 8192;
#pragma unroll
  for (int t = 0; t < 2; ++t) {
    u4 o;
#pragma unroll
    for (int r = 0; r < 4; ++r) {
      const float a = re[t][r], b = im[t][r], c = c2[4 * t + r], s = s2[4 * t + r];
      o[r] = pk_h(a * c - b * s, -(a * s + b * c));
    }
    *(u4*)(img2 + (16 * t + n) * 256 + (((4 * w + g) ^ n) << 4)) = o;
  }
  __syncthreads();
  dft64(img2, n, g, bc, bs, re, im);
#pragma unroll
  for (int t = 0; t < 2; ++t) {
    u4 o;
#pragma unroll
    for (int r = 0; r < 4; ++r) o[r] = pk_h(re[t][r], im[t][r]);
    Y1T[k2 * 512 + 8 * (16 * w + n) + 4 * t + g] = o;
  }
}

__global__ __launch_bounds__(256) void k3_rowifft(const unsigned char* __restrict__ Y1T, const unsigned* __restrict__ x,
                                                  unsigned* __restrict__ h, unsigned* __restrict__ hn) {
  __shared__ __attribute__((aligned(16))) unsigned char lds[8192];
  const int b = blockIdx.x, tid = threadIdx.x;
  const int q = (b & 7) * 32 + (b >> 3);
  const int w = __builtin_amdgcn_readfirstlane(tid >> 6), lane = tid & 63, n = lane & 15, g = lane >> 4;
#pragma unroll
  for (int i = 0; i < 2; ++i) {
    const int kk = 64 * w + 32 * i + (lane >> 1);
    const int k2 = kk < 224 ? kk : 224;
    const int hf = (lane & 1) ^ ((kk >> 2) & 1);
    glds16(Y1T + k2 * 8192 + q * 32 + hf * 16, lds + (2 * w + i) * 1024);
  }
  const int off = (8 * q + 2 * w + (g >> 1)) * 224 + (g & 1) * 112 + n;
  unsigned xv[7];
#pragma unroll
  for (int j = 0; j < 7; ++j) xv[j] = x[off + 16 * j];
  h8 tb3[10];
#pragma unroll
  for (int i = 0; i < 10; ++i) tb3[i] = ldfrag(&TAB3.v[i][0][0], lane);
  float tc[8], ts[8];
#pragma unroll
  for (int i = 0; i < 8; ++i) {
    const float a = (float)((2 * n + (i >> 2)) * (4 * (g & 1) + (i & 3))) * (1.0f / 448.0f);
    tc[i] = __builtin_amdgcn_cosf(a); ts[i] = __builtin_amdgcn_sinf(a);
  }
  const int ka = n & 7, rowb = (2 * (w & 1) + (n >> 3)) * 4, hsel = w >> 1;
  int ad[8];
#pragma unroll
  for (int i = 0; i < 8; ++i) {
    const int kf = ka + 14 * (4 * g + (i & 3));
    const int k2 = (i < 4) ? kf : 224 - kf;
    ad[i] = k2 * 32 + ((hsel ^ ((k2 >> 2) & 1)) << 4) + rowb;
  }
  asm volatile("s_waitcnt vmcnt(0)" ::: "memory");
  __syncthreads();
  unsigned dv[8];
#pragma unroll
  for (int i = 0; i < 8; ++i) dv[i] = *(const unsigned*)(lds + ad[i]);
  const h8 a0 = mk_h8(dv[0], dv[1], dv[2], dv[3]), a1 = mk_h8(dv[4], dv[5], dv[6], dv[7]);
  const f4 z = {0.f, 0.f, 0.f, 0.f};
  f4 y[2][2];
#pragma unroll
  for (int t = 0; t < 2; ++t) {
    const f4 vr = MFMA16H(a1, tb3[4 * t + 1], MFMA16H(a0, tb3[4 * t], z));
    const f4 vi = MFMA16H(a1, tb3[4 * t + 3], MFMA16H(a0, tb3[4 * t + 2], z));
    float pr[4], pi[4];
#pragma unroll
    for (int r = 0; r < 4; ++r) {
      const float c = tc[4 * t + r], s = ts[4 * t + r];
      pr[r] = vr[r] * c - vi[r] * s; pi[r] = vr[r] * s + vi[r] * c;
    }
    const h8 bb = mk_h8(pk_h(pr[0], pr[1]), pk_h(pr[2], pr[3]), pk_h(pi[0], pi[1]), pk_h(pi[2], pi[3]));
    y[t][0] = MFMA16H(tb3[8], bb, z); y[t][1] = MFMA16H(tb3[9], bb, z);
  }
  float v[14], sm = 0.f;
#pragma unroll
  for (int j = 0; j < 7; ++j) {
    const unsigned hp = pk_bf(bflo(xv[j]) + bfr(y[0][j >> 2][j & 3] * (1.0f / 224.0f)),
                              bfhi(xv[j]) + bfr(y[1][j >> 2][j & 3] * (1.0f / 224.0f)));
    h[off + 16 * j] = hp;
    v[2 * j] = bflo(hp); v[2 * j + 1] = bfhi(hp); sm += v[2 * j] + v[2 * j + 1];
  }
  const float mean = sum32(sm) * (1.0f / 448.0f);
  float sq = 0.f;
#pragma unroll
  for (int j = 0; j < 14; ++j) { const float d = v[j] - mean; sq += d * d; }
  const float var = sum32(sq) * (1.0f / 448.0f);
  const float mb = bfr(mean);
  const float rs = 1.0f / bfr(sqrtf(bfr(bfr(var) + 1e-5f)));
  const float m2 = bfr((mean - mb) * rs);
  const float rs2 = 1.0f / bfr(sqrtf(bfr(bfr(var * rs * rs) + 1e-5f)));
  const float al = rs * rs2, be = -m2 * rs2;
#pragma unroll
  for (int j = 0; j < 7; ++j) hn[off + 16 * j] = pk_bf((v[2 * j] - mb) * al + be, (v[2 * j + 1] - mb) * al + be);
}

constexpr int NS = 8;
DI b8 trfrag(const unsigned char* p) {
  const s4 lo = __builtin_amdgcn_ds_read_tr16_b64_v4i16((LDSP s4*)p);
  const s4 hi = __builtin_amdgcn_ds_read_tr16_b64_v4i16((LDSP s4*)(p + 512));
  return __builtin_bit_cast(b8, __builtin_shufflevector(lo, hi, 0, 1, 2, 3, 4, 5, 6, 7));
}
DI u4 make_srd(const void* p, unsigned bytes) {
  const unsigned long long a = (unsigned long long)p;
  u4 r = {(unsigned)a, (unsigned)(a >> 32) & 0xffffu, bytes, 0x00020000u};
  return r;
}
DI void dma16(const u4& srd, unsigned voff, unsigned soff, unsigned ldsdst) {
  asm volatile("s_mov_b32 m0, %3\n\ts_nop 0\n\tbuffer_load_dwordx4 %1, %0, %2 offen lds"
               ::"s"(srd), "v"(voff), "s"(soff), "s"(ldsdst) : "memory");
}
template <int N> DI void wait_step() {
  asm volatile("" ::: "memory");
  __builtin_amdgcn_s_waitcnt(((N >> 4) << 14) | (N & 15) | 0x70);
  __builtin_amdgcn_s_barrier();
  asm volatile("" ::: "memory");
  __builtin_amdgcn_sched_barrier(0);
}
template <int K, int N, int EPI>
__global__ __launch_bounds__(256) void k_gemm(const unsigned char* __restrict__ A, const unsigned char* __restrict__ B,
                                              const unsigned short* bias, u2* C) {
  __shared__ __attribute__((aligned(16))) unsigned char lds[NS * 16384];
  constexpr int NT = K / 64, NB = 7 * (N / 64), PER = (NB + 7) / 8;
  constexpr unsigned SA = 64 * D_ * 2, SB = 64 * N * 2;
  const int b = blockIdx.x, tid = threadIdx.x;
  const int L = (b & 7) * PER + (b >> 3);
  if (L >= NB) return;
  const int nt = L / 7, dt = L - nt * 7;
  const int w = __builtin_amdgcn_readfirstlane(tid >> 6), lane = tid & 63;
  const int wm = w & 1, wn = w >> 1, hh = lane >> 5;
  const int ncol = nt * 64 + wn * 32 + (lane & 31);
  u2* cp = C + (ncol * 448 + dt * 64 + wm * 32 + 4 * hh) / 4;
  const unsigned braw = bias[ncol];
  u2 res[4];
  if (EPI == 1) {
#pragma unroll
    for (int i = 0; i < 4; ++i) res[i] = cp[2 * i];
  }
  const int srow = 16 * w + (lane >> 3), sch = ((lane & 7) ^ (((lane >> 4) & 1) << 2)) << 4;
  const unsigned va = srow * (D_ * 2) + dt * 128 + sch, vb = srow * (N * 2) + nt * 128 + sch;
  const unsigned lw = (unsigned)(size_t)(LDSP unsigned char*)lds + w * 2048;
  auto piece = [&](int t, int i) {
    const unsigned l = lw + (t % NS) * 16384;
    const u4 srdA = make_srd(A, t < NT ? K * D_ * 2 : 0), srdB = make_srd(B, t < NT ? K * N * 2 : 0);
    if (i == 0) dma16(srdB, vb, t * SB, l + 8192);
    if (i == 1) dma16(srdB, vb, t * SB + 8 * N * 2, l + 8192 + 1024);
    if (i == 2) dma16(srdA, va, t * SA, l);
    if (i == 3) dma16(srdA, va, t * SA + 8 * D_ * 2, l + 1024);
  };
#pragma unroll
  for (int t = 0; t < NS; ++t) {
#pragma unroll
    for (int i = 0; i < 4; ++i) piece(t, i);
  }
  const int q = (lane & 15) >> 2;
  const int lb = (8 * hh + q) * 128 + ((lane >> 4) & 1) * 32 + (lane & 3) * 8;
  const unsigned char* ra = lds + lb + ((wm ^ (q >> 1)) << 6);
  const unsigned char* rb = lds + 8192 + lb + ((wn ^ (q >> 1)) << 6);
  f16v acc = {};
  b8 fa[2][4], fb[2][4];
  wait_step<4 * (NS - 1)>();
#pragma unroll
  for (int ks = 0; ks < 4; ++ks) { fa[0][ks] = trfrag(ra + ks * 2048); fb[0][ks] = trfrag(rb + ks * 2048); }
  for (int t0 = 0; t0 < NT; t0 += 2) {
#pragma unroll
    for (int u = 0; u < 2; ++u) {
      const int t = t0 + u;
      wait_step<4 * (NS - 2)>();
      const int so = ((t + 1) % NS) * 16384;
#pragma unroll
      for (int ks = 0; ks < 4; ++ks) {
        acc = __builtin_amdgcn_mfma_f32_32x32x16_bf16(fa[u][ks], fb[u][ks], acc, 0, 0, 0);
        piece(t + NS, ks);
        fa[u ^ 1][ks] = trfrag(ra + so + ks * 2048); fb[u ^ 1][ks] = trfrag(rb + so + ks * 2048);
        __builtin_amdgcn_sched_barrier(0);
      }
    }
  }
  const float bv = bflo(braw);
#pragma unroll
  for (int i = 0; i < 4; ++i) {
    float r[4];
#pragma unroll
    for (int j = 0; j < 4; ++j) {
      const float u = bfr(bfr(acc[4 * i + j]) + bv);
      if (EPI == 0) {
        const float zz = u * (1.0f + 0.044715f * u * u) * (-2.0f * 0.7978845608028654f * 1.4426950408889634f);
        r[j] = u * __builtin_amdgcn_rcpf(1.0f + __builtin_amdgcn_exp2f(zz));
      } else {
        const unsigned rv = res[i][j >> 1];
        r[j] = u + ((j & 1) ? bfhi(rv) : bflo(rv));
      }
    }
    u2 o = {pk_bf(r[0], r[1]), pk_bf(r[2], r[3])};
    cp[2 * i] = o;
  }
}

extern "C" void kernel_launch(void* const* d_in, const int* in_sizes, int n_in, void* d_out, int out_size, void* d_ws,
                              size_t ws_size, hipStream_t stream) {
  const void* x = d_in[0];
  const void* wr = d_in[1];
  const void* wi = d_in[2];
  const void* w1 = d_in[4];
  const void* b1 = d_in[5];
  const void* w2 = d_in[6];
  const void* b2 = d_in[7];
  unsigned char* ws = (unsigned char*)d_ws;
  unsigned char* X1T = ws;
  unsigned char* WT = ws + (2u << 20);
  unsigned char* Y1T = ws + (4u << 20);
  unsigned char* hn = ws + (6u << 20);
  unsigned char* uT = ws + (8u << 20);
  k1_rowfft<<<256, 512, 0, stream>>>((const unsigned*)x, (const u4*)wr, (const u4*)wi, (u4*)X1T, (u4*)WT);
  k2_colfft<<<225, 256, 0, stream>>>(X1T, (const unsigned*)WT, (u4*)Y1T);
  k3_rowifft<<<256, 256, 0, stream>>>(Y1T, (const unsigned*)x, (unsigned*)d_out, (unsigned*)hn);
  k_gemm<S_, F_, 0><<<200, 256, 0, stream>>>(hn, (const unsigned char*)w1, (const unsigned short*)b1, (u2*)uT);
  k_gemm<F_, S_, 1><<<224, 256, 0, stream>>>(uT, (const unsigned char*)w2, (const unsigned short*)b2, (u2*)d_out);
}
```

```cpp
#include <hip/hip_runtime.h>
#include <stdint.h>

typedef _Float16 h8 __attribute__((ext_vector_type(8)));
typedef _Float16 h2 __attribute__((ext_vector_type(2)));
typedef __bf16 b8 __attribute__((ext_vector_type(8)));
typedef __bf16 b2 __attribute__((ext_vector_type(2)));
typedef short s4 __attribute__((ext_vector_type(4)));
typedef short s8 __attribute__((ext_vector_type(8)));
typedef float f2 __attribute__((ext_vector_type(2)));
typedef float f4 __attribute__((ext_vector_type(4)));
typedef float f16v __attribute__((ext_vector_type(16)));
typedef unsigned u2 __attribute__((ext_vector_type(2)));
typedef unsigned u4 __attribute__((ext_vector_type(4)));
#define DI __device__ __forceinline__
#define LDSP __attribute__((address_space(3)))

constexpr int S_ = 2048, D_ = 448, DF_ = 225, F_ = 1792;

namespace tb {
constexpr double PI = 3.14159265358979323846264338327950288;
constexpr double cos_rev(int num, int den) {
  num %= den; if (num < 0) num += den;
  if (2 * num > den) num = den - num;
  if (4 * num == den) return 0.0;
  double sg = 1.0, a = 2.0 * PI * num / den;
  if (4 * num > den) { sg = -1.0; a = PI * (den - 2 * num) / den; }
  double t = 1.0, s = 1.0, a2 = a * a;
  for (int i = 1; i <= 13; ++i) { t = -t * a2 / ((2 * i - 1) * (2 * i)); s += t; }
  return sg * s;
}
constexpr unsigned short f2h(double v) {
  if (v == 0.0) return 0;
  unsigned sg = v < 0 ? 0x8000u : 0u; if (v < 0) v = -v;
  int e = 0; double p = 1.0;
  while (v >= 2.0 * p) { p *= 2.0; ++e; }
  while (v < p) { p *= 0.5; --e; }
  if (e < -14) { e = -14; p = 1.0 / 16384.0; }
  double q = v / p * 1024.0;
  long qi = (long)q; double fr = q - (double)qi;
  if (fr > 0.5 || (fr == 0.5 && (qi & 1))) ++qi;
  long bits = qi < 1024 ? qi : (long)((e + 15) << 10) + (qi - 1024);
  return (unsigned short)(sg | (unsigned)bits);
}
struct Base { unsigned short c1[448], cq[448], c2[448], c8[64]; };
constexpr Base make_base() {
  Base b{};
  for (int i = 0; i < 448; ++i) { double c = cos_rev(i, 448); b.c1[i] = f2h(c); b.cq[i] = f2h(0.25 * c); b.c2[i] = f2h(2.0 * c); }
  for (int i = 0; i < 64; ++i) b.c8[i] = f2h(0.125 * cos_rev(i, 64));
  return b;
}
constexpr Base BASE = make_base();
constexpr int m448(int m) { return ((m % 448) + 448) % 448; }
constexpr unsigned short neg(unsigned short v) { return (unsigned short)(v ^ 0x8000u); }
struct T1 { unsigned short v[8][64][8]; };
constexpr T1 make_t1() {
  T1 t{};
  for (int l = 0; l < 64; ++l) for (int j = 0; j < 8; ++j) {
    const int n = l & 15, g = l >> 4;
    {
      const int r = n >> 3, ka = n & 7, rp = g >> 1, d1 = 7 * (g & 1) + j;
      const bool on = (r == rp) && j < 7;
      t.v[0][l][j] = on ? BASE.c1[m448(32 * d1 * ka)] : 0;
      t.v[1][l][j] = on ? neg(BASE.c1[m448(32 * d1 * ka - 112)]) : 0;
    }
    for (int tt = 0; tt < 2; ++tt) {
      const int kb = 16 * tt + n, d2 = 8 * g + j;
      const unsigned short c = BASE.cq[m448(14 * d2 * kb)], s = BASE.cq[m448(14 * d2 * kb - 112)];
      t.v[2 + 3 * tt][l][j] = c; t.v[3 + 3 * tt][l][j] = s; t.v[4 + 3 * tt][l][j] = tt ? neg(c) : neg(s);
    }
  }
  return t;
}
struct T2 { unsigned short a[4][4][64][8]; };
constexpr T2 make_t2() {
  T2 t{};
  for (int w = 0; w < 4; ++w) for (int ks = 0; ks < 4; ++ks) for (int l = 0; l < 64; ++l) for (int jj = 0; jj < 4; ++jj) {
    const int k1a = 16 * w + (l & 15), s1 = 16 * ks + 4 * (l >> 4) + jj, m = s1 * k1a;
    t.a[w][ks][l][2 * jj] = BASE.c8[m & 63];
    t.a[w][ks][l][2 * jj + 1] = BASE.c8[(m - 16) & 63];
  }
  return t;
}
struct T2b { unsigned short b[2][3][64][8]; };
constexpr T2b make_t2b() {
  T2b t{};
  for (int tt = 0; tt < 2; ++tt) for (int l = 0; l < 64; ++l) for (int j = 0; j < 8; ++j) {
    const int kk = 16 * tt + (l & 15), ss = 16 * (j >> 2) + 4 * (l >> 4) + (j & 3);
    const unsigned short c = BASE.cq[m448(14 * kk * ss)], s = BASE.cq[m448(14 * kk * ss - 112)];
    t.b[tt][0][l][j] = c; t.b[tt][1][l][j] = s; t.b[tt][2][l][j] = neg(s);
  }
  return t;
}
struct T3 { unsigned short v[10][64][8]; };
constexpr T3 make_t3() {
  T3 t{};
  for (int l = 0; l < 64; ++l) for (int j = 0; j < 8; ++j) {
    const int n = l & 15, g = l >> 4;
    for (int tt = 0; tt < 2; ++tt) for (int ks = 0; ks < 2; ++ks) {
      const int kb = 16 * ks + 4 * g + (j >> 1), part = j & 1, d2 = 2 * n + tt;
      const unsigned short c = BASE.cq[m448(14 * d2 * kb)], s = BASE.cq[m448(14 * d2 * kb - 112)];
      t.v[(tt * 2 + 0) * 2 + ks][l][j] = part == 0 ? c : (ks == 0 ? s : neg(s));
      t.v[(tt * 2 + 1) * 2 + ks][l][j] = part == 0 ? s : (ks == 0 ? neg(c) : c);
    }
    for (int u = 0; u < 2; ++u) {
      const int gp = n >> 2, rg = n & 3, e = 4 * u + rg, d1 = 7 * (gp & 1) + e;
      const int ka = 4 * (g & 1) + (j & 3), part = j >> 2;
      const bool on = ((gp >> 1) == (g >> 1)) && e < 7;
      const bool one = (ka == 0 || ka == 7);
      const int idx = m448(32 * d1 * ka - (part ? 112 : 0));
      unsigned short val = one ? BASE.c1[idx] : BASE.c2[idx];
      if (part) val = neg(val);
      t.v[8 + u][l][j] = on ? val : 0;
    }
  }
  return t;
}
}
__device__ __attribute__((aligned(16))) const tb::T1 TAB1 = tb::make_t1();
struct TabM { tb::T2 a; tb::T2b b; tb::T3 c; };
__device__ __attribute__((aligned(16))) const TabM TABM = {tb::make_t2(), tb::make_t2b(), tb::make_t3()};

#define PF2 5
#define STAG 0
#define EXP 0
#define STAMP(k, i)
#define STAMP_END(k, i)
DI float bfr(float x) { return (float)(__bf16)x; }
DI float bflo(unsigned v) { return __uint_as_float(v << 16); }
DI float bfhi(unsigned v) { return __uint_as_float(v & 0xffff0000u); }
DI unsigned pk_bf(float a, float b) { f2 v = {a, b}; return __builtin_bit_cast(unsigned, __builtin_convertvector(v, b2)); }
DI unsigned pk_h(float a, float b) { f2 v = {a, b}; return __builtin_bit_cast(unsigned, __builtin_convertvector(v, h2)); }
DI h8 mk_h8(unsigned a, unsigned b, unsigned c, unsigned d) { u4 v = {a, b, c, d}; return __builtin_bit_cast(h8, v); }
template <int C> DI float dpp_add(float v) {
  return v + __uint_as_float((unsigned)__builtin_amdgcn_update_dpp(0, (int)__float_as_uint(v), C, 0xf, 0xf, false));
}
DI float sum32(float v) {
  v = dpp_add<0x128>(v); v = dpp_add<0x124>(v); v = dpp_add<0x122>(v); v = dpp_add<0x121>(v);
  auto r = __builtin_amdgcn_permlane16_swap(__float_as_uint(v), __float_as_uint(v), false, false);
  return __uint_as_float(r[0]) + __uint_as_float(r[1]);
}
template <typename T, int N> DI void pin(T (&a)[N]) {
#pragma unroll
  for (int i = 0; i < N; ++i) asm volatile("" : "+v"(a[i]));
}
#define MFMA16H(a, b, c) __builtin_amdgcn_mfma_f32_16x16x32_f16((a), (b), (c), 0, 0, 0)
DI h8 ldfrag(const unsigned short* p, int lane) { return *(const h8*)(p + lane * 8); }
DI void glds16(const void* g, void* l) {
  __builtin_amdgcn_global_load_lds((const __attribute__((address_space(1))) void*)g, (LDSP void*)l, 16, 0, 0);
}

DI void lds_barrier() {
  asm volatile("" ::: "memory");
  __builtin_amdgcn_s_waitcnt(0xC07F);
  __builtin_amdgcn_s_barrier();
  asm volatile("" ::: "memory");
}
constexpr unsigned OFF_X1T = 0, OFF_WT = 2u << 20, OFF_Y1T = 4u << 20, OFF_HN = 6u << 20, OFF_UT = 8u << 20,
                   OFF_H = 10u << 20, OFF_FLG = 12u << 20, WS_BYTES = 13u << 20;

__global__ __launch_bounds__(512) void k1_rowfft(const unsigned* __restrict__ x, const u4* __restrict__ wr,
                                                 const u4* __restrict__ wi, unsigned char* __restrict__ ws,
                                                 const unsigned char* __restrict__ w1) {
  __shared__ __attribute__((aligned(16))) unsigned lds[2 * 928 + 2 * 904];
  const int b = blockIdx.x, tid = threadIdx.x;
  u4* X1T = (u4*)(ws + OFF_X1T);
  u4* WT = (u4*)(ws + OFF_WT);
  const int q = (b & 7) * 32 + (b >> 3);
  STAMP(0, 0);
  if (tid < 256) {
    const int wv = tid >> 6, lane = tid & 63, n = lane & 15, g = lane >> 4;
    const int s = 32 * (8 * (q & 7) + 2 * wv + (g >> 1)) + (q >> 3);
    const unsigned* xp = x + s * 224 + (g & 1) * 112 + n;
    unsigned xv[7];
#pragma unroll
    for (int j = 0; j < 7; ++j) xv[j] = xp[16 * j];
    h8 tb1[8];
#pragma unroll
    for (int i = 0; i < 8; ++i) tb1[i] = ldfrag(&TAB1.v[i][0][0], lane);
    __builtin_amdgcn_sched_barrier(0);
    const int ka = n & 7;
    float tc[8], ts[8];
#pragma unroll
    for (int i = 0; i < 8; ++i) {
      const float a = (float)((8 * g + i) * ka) * (1.0f / 448.0f);
      tc[i] = 0.25f * __builtin_amdgcn_cosf(a); ts[i] = 0.25f * __builtin_amdgcn_sinf(a);
    }
    pin(tc); pin(ts); pin(xv);
    float v[14], sm = 0.f;
#pragma unroll
    for (int j = 0; j < 7; ++j) { v[2 * j] = bflo(xv[j]); v[2 * j + 1] = bfhi(xv[j]); sm += v[2 * j] + v[2 * j + 1]; }
    const float mean = sum32(sm) * (1.0f / 448.0f);
    STAMP(0, 1);
    float sq = 0.f;
#pragma unroll
    for (int j = 0; j < 14; ++j) { const float d = v[j] - mean; sq += d * d; }
    const float var = sum32(sq) * (1.0f / 448.0f);
    const float rs = 1.0f / bfr(sqrtf(bfr(bfr(var) + 1e-5f)));
    const float mb = bfr(mean);
#pragma unroll
    for (int j = 0; j < 14; ++j) v[j] = (v[j] - mb) * rs;
    const f4 z = {0.f, 0.f, 0.f, 0.f};
    f4 tre[2], tim[2];
#pragma unroll
    for (int t = 0; t < 2; ++t) {
      const h8 a = mk_h8(pk_h(v[t], v[2 + t]), pk_h(v[4 + t], v[6 + t]), pk_h(v[8 + t], v[10 + t]), pk_h(v[12 + t], 0.f));
      tre[t] = MFMA16H(a, tb1[0], z); tim[t] = MFMA16H(a, tb1[1], z);
    }
    float pr[8], pi[8];
#pragma unroll
    for (int i = 0; i < 8; ++i) {
      const float a = tre[i & 1][i >> 1], bb = tim[i & 1][i >> 1];
      pr[i] = a * tc[i] + bb * ts[i]; pi[i] = bb * tc[i] - a * ts[i];
    }
    const h8 bre = mk_h8(pk_h(pr[0], pr[1]), pk_h(pr[2], pr[3]), pk_h(pr[4], pr[5]), pk_h(pr[6], pr[7]));
    const h8 bim = mk_h8(pk_h(pi[0], pi[1]), pk_h(pi[2], pi[3]), pk_h(pi[4], pi[5]), pk_h(pi[6], pi[7]));
    const f4 xr0 = MFMA16H(tb1[3], bim, MFMA16H(tb1[2], bre, z));
    const f4 xi0 = MFMA16H(tb1[2], bim, MFMA16H(tb1[4], bre, z));
    const f4 xr1 = MFMA16H(tb1[6], bim, MFMA16H(tb1[5], bre, z));
    const f4 xi1 = MFMA16H(tb1[7], bim, MFMA16H(tb1[6], bre, z));
    unsigned* o = lds + (wv >> 1) * 928 + 2 * (wv & 1) + (n >> 3);
    const int k0 = ka + 56 * g, k1 = 224 - k0;
#pragma unroll
    for (int r = 0; r < 4; ++r) o[4 * (k0 + 14 * r)] = pk_h(xr0[r], xi0[r]);
#pragma unroll
    for (int r = 0; r < 4; ++r) o[4 * (k1 - 14 * r)] = pk_h(xr1[r], xi1[r]);
    __syncthreads();
    STAMP(0, 2);
#pragma unroll
    for (int p = 0; p < 2; ++p) {
      const int i = tid + 256 * p;
      if (i < 450) {
        const int k2 = i >> 1, hf = i & 1;
        X1T[k2 * 512 + q * 2 + hf] = *(const u4*)(lds + hf * 928 + k2 * 4);
      }
    }
    STAMP_END(0, 3);
  } else {
    const int t = tid - 256;
    unsigned* lw = lds + 2 * 928;
    const int tt = t < 225 ? t : 224;
    const u4 a = wr[q * 225 + tt], c = wi[q * 225 + tt];
    int pr = ((b >> 3) * 2 + (b & 1)) * 18 + ((t >> 3) < 18 ? (t >> 3) : 17);
    const int pl = (t & 7) < 7 ? (t & 7) : 6;
    const unsigned pf0 = *(const unsigned*)(w1 + ((b >> 1) & 3) * 896 + pl * 128 + (size_t)((pr & 3) * 512 + (pr >> 2)) * (F_ * 2));
    {
      const u4 ones = {~0u, ~0u, ~0u, ~0u}, zero = {0u, 0u, 0u, 0u};
      u4* y = (u4*)(ws + OFF_Y1T) + b * 450;
      y[t] = ones;
      if (t < 194) y[256 + t] = ones;
      if (t >= 240) ((u4*)(ws + OFF_FLG))[b * 16 + (t - 240)] = zero;
    }
    *(u4*)(lw + 4 * tt) = a; *(u4*)(lw + 904 + 4 * tt) = c;
    lds_barrier();
    const unsigned short* lr = (const unsigned short*)lw;
#pragma unroll
    for (int p = 0; p < 2; ++p) {
      const int i = t + 256 * p;
      if (i < 450) {
        const int k2 = i >> 1, hf = i & 1;
        u4 o;
#pragma unroll
        for (int r = 0; r < 4; ++r) {
          const int e = (4 * hf + r) * 225 + k2;
          o[r] = (unsigned)lr[e] | ((unsigned)lr[1808 + e] << 16);
        }
        WT[k2 * 512 + q * 2 + hf] = o;
      }
    }
    asm volatile("" ::"v"(pf0));
  }
}

DI b8 trfrag(const unsigned char* p) {
  const s4 lo = __builtin_amdgcn_ds_read_tr16_b64_v4i16((LDSP s4*)p);
  const s4 hi = __builtin_amdgcn_ds_read_tr16_b64_v4i16((LDSP s4*)(p + 512));
  return __builtin_bit_cast(b8, __builtin_shufflevector(lo, hi, 0, 1, 2, 3, 4, 5, 6, 7));
}
DI u4 make_srd(const void* p, unsigned bytes) {
  const unsigned long long a = (unsigned long long)p;
  u4 r = {(unsigned)a, (unsigned)(a >> 32) & 0xffffu, bytes, 0x00020000u};
  return r;
}
DI void dma16(const u4& srd, unsigned voff, unsigned soff, unsigned ldsdst) {
  asm volatile("s_mov_b32 m0, %3\n\ts_nop 0\n\tbuffer_load_dwordx4 %1, %0, %2 offen lds"
               ::"s"(srd), "v"(voff), "s"(soff), "s"(ldsdst) : "memory");
}
template <typename T> DI void ld16(T& dst, const u4& srd, unsigned voff, unsigned soff) {
  asm volatile("buffer_load_dwordx4 %0, %2, %1, %3 offen" : "=v"(dst) : "s"(srd), "v"(voff), "s"(soff) : "memory");
}
template <typename T> DI void ld16c(T& dst, const u4& srd, unsigned voff, unsigned soff) {
  asm volatile("buffer_load_dwordx4 %0, %2, %1, %3 offen sc1" : "=v"(dst) : "s"(srd), "v"(voff), "s"(soff) : "memory");
}
template <int OFF> DI void ld8(u2& dst, const u4& srd, unsigned voff) {
  asm volatile("buffer_load_dwordx2 %0, %2, %1, 0 offen offset:%3" : "=v"(dst) : "s"(srd), "v"(voff), "n"(OFF) : "memory");
}
template <int OFF> DI void ld8c(u2& dst, const u4& srd, unsigned voff) {
  asm volatile("buffer_load_dwordx2 %0, %2, %1, 0 offen offset:%3 sc1" : "=v"(dst) : "s"(srd), "v"(voff), "n"(OFF) : "memory");
}
template <int OFF> DI void ld4(unsigned& dst, const u4& srd, unsigned voff, unsigned soff) {
  asm volatile("buffer_load_dword %0, %2, %1, %3 offen offset:%4" : "=v"(dst) : "s"(srd), "v"(voff), "s"(soff), "n"(OFF) : "memory");
}
DI void st16c(const u4& v, const u4& srd, unsigned voff) {
  asm volatile("buffer_store_dwordx4 %0, %2, %1, 0 offen sc1\n\ts_nop 1" ::"v"(v), "s"(srd), "v"(voff) : "memory");
}
template <int OFF> DI void st8c(const u2& v, const u4& srd, unsigned voff, unsigned soff) {
  asm volatile("buffer_store_dwordx2 %0, %2, %1, %3 offen offset:%4 sc1" ::"v"(v), "s"(srd), "v"(voff), "s"(soff), "n"(OFF) : "memory");
}
template <int N> DI void wait_vm() { asm volatile("s_waitcnt vmcnt(%0)" ::"n"(N) : "memory"); }
template <int N> DI void wait_vm(b8& a, b8& b) { asm volatile("s_waitcnt vmcnt(%2)" : "+v"(a), "+v"(b) : "n"(N) : "memory"); }
DI void wait_vm_n(int n, b8& a, b8& b) {
  switch (n) {
#define WV(i) case i: wait_vm<2 * i>(a, b); break;
    WV(0) WV(1) WV(2) WV(3) WV(4) WV(5) WV(6) WV(7) WV(8) WV(9) WV(10) WV(11) WV(12) WV(13) WV(14) WV(15)
#undef WV
  }
}
constexpr unsigned SPIN_CAP = 1u << 16;
DI void poll_flag(const u4& srd, unsigned voff) {
  for (unsigned sp = 0; sp < SPIN_CAP; ++sp) {
    unsigned f;
    asm volatile("buffer_load_dword %0, %2, %1, 0 offen sc1\n\ts_waitcnt vmcnt(0)" : "=v"(f) : "s"(srd), "v"(voff) : "memory");
    if (__builtin_amdgcn_ballot_w64(f == 0u) == 0ull) break;
  }
}
DI void dft64(const unsigned char* img, int n, int g, const h8 (&bc)[4], const h8 (&bs)[4], f4 (&re)[2], f4 (&im)[2]) {
  const f4 z = {0.f, 0.f, 0.f, 0.f};
#pragma unroll
  for (int t = 0; t < 2; ++t) {
    re[t] = z; im[t] = z;
#pragma unroll
    for (int ks = 0; ks < 4; ++ks) {
      const h8 a = *(const h8*)(img + (16 * t + n) * 256 + (((4 * ks + g) ^ n) << 4));
      re[t] = MFMA16H(a, bc[ks], re[t]); im[t] = MFMA16H(a, bs[ks], im[t]);
    }
  }
}
#define MFMA32B(a, b, c) __builtin_amdgcn_mfma_f32_32x32x16_bf16((a), (b), (c), 0, 0, 0)
#define GWV 8
constexpr int GW = GWV;
constexpr int NSL = 18, WREG = NSL * 2048;
constexpr int FFTA = 4 * WREG;
constexpr int NC1 = 18, NC2 = 14, NH2 = 14;
constexpr unsigned SA = 2 * D_ * 16;

DI unsigned red_off(int dst, int i) { return i < 3 ? dst * WREG + (NH2 + 2 * (i - 1)) * 2048 : FFTA + dst * 4096; }
DI void reduce_acc(unsigned char* lds, int w, int lane, f16v (&acc)[4], float (&sum)[16]) {
#pragma unroll
  for (int i = 1; i < 4; ++i) {
    f4* red = (f4*)(lds + red_off(w ^ i, i)) + lane;
#pragma unroll
    for (int r = 0; r < 4; ++r) {
      const f4 o = {acc[i][4 * r], acc[i][4 * r + 1], acc[i][4 * r + 2], acc[i][4 * r + 3]};
      red[r * 64] = o;
    }
  }
  lds_barrier();
#pragma unroll
  for (int r = 0; r < 4; ++r) {
    const f4 p0 = ((const f4*)(lds + red_off(w, 1)))[r * 64 + lane], p1 = ((const f4*)(lds + red_off(w, 2)))[r * 64 + lane],
             p2 = ((const f4*)(lds + red_off(w, 3)))[r * 64 + lane];
#pragma unroll
    for (int j = 0; j < 4; ++j) sum[4 * r + j] = (acc[0][4 * r + j] + p0[j]) + (p1[j] + p2[j]);
  }
}

__global__ __launch_bounds__(256, 1) void k2_mega(unsigned char* ws, const unsigned char* x, const unsigned char* w1,
                                                  const unsigned char* b1, const unsigned char* w2,
                                                  const unsigned char* b2, u2* out) {
  __shared__ __attribute__((aligned(16))) unsigned char lds[163840];
  const int b = blockIdx.x, tid = threadIdx.x;
  const int w = __builtin_amdgcn_readfirstlane(tid >> 6), lane = tid & 63, n = lane & 15, g = lane >> 4;
  const int xc = b & 7, rk = b >> 3;
  const bool has1 = rk < 28;
  const int dt = 4 * (xc & 1) + (rk & 3), nt1 = 7 * (xc >> 1) + (rk >> 2), nt2 = 8 * (xc >> 1) + (rk >> 2);
  const u4 sws = make_srd(ws, WS_BYTES), stab = make_srd(&TABM, sizeof(TabM));
  const u4 sw1 = make_srd(w1 + w * (512 * F_ * 2), 512 * F_ * 2), sw2 = make_srd(w2 + w * (448 * S_ * 2), 448 * S_ * 2);
  const unsigned lbase = (unsigned)(size_t)(LDSP unsigned char*)lds;
  const unsigned lw = lbase + w * WREG;
  const unsigned swz = ((lane & 7) ^ (((lane >> 4) & 1) << 2)) << 4;
  const unsigned vb1 = (lane >> 3) * (F_ * 2) + nt1 * 128 + swz, vb2 = (lane >> 3) * (S_ * 2) + nt2 * 128 + swz;
  const int wm = w & 1, wn = w >> 1, hh = lane >> 5, l31 = lane & 31, q4 = n >> 2;
  const int lb = w * WREG + (8 * hh + q4) * 128 + (g & 1) * 32 + (lane & 3) * 8;
  const unsigned char* rb0 = lds + lb + ((wn ^ (q4 >> 1)) << 6);
  const unsigned char* rb1 = lds + lb + ((wn ^ 1 ^ (q4 >> 1)) << 6);
  unsigned char* fft = lds + FFTA;
  const f4 z = {0.f, 0.f, 0.f, 0.f};
  STAMP(1, 0);

  if (b < DF_) {
#pragma unroll
    for (int i = 0; i < 2; ++i) {
      const int row = 8 * w + 4 * i + g;
      dma16(sws, row * 256 + ((n ^ (row & 15)) << 4), OFF_X1T + b * 8192, lbase + FFTA + (8 * w + 4 * i) * 256);
    }
    h8 bc[4], bs[4], fb[6];
#pragma unroll
    for (int i = 0; i < 4; ++i) ld16(bc[i], stab, lane * 16, (w * 4 + i) * 1024);
#pragma unroll
    for (int i = 0; i < 6; ++i) ld16(fb[i], stab, lane * 16, 16384 + i * 1024);
    unsigned wv[8];
    const unsigned vw = (256 * g + 16 * w + n) * 4;
    ld4<0>(wv[0], sws, vw, OFF_WT + b * 8192); ld4<256>(wv[1], sws, vw, OFF_WT + b * 8192);
    ld4<512>(wv[2], sws, vw, OFF_WT + b * 8192); ld4<768>(wv[3], sws, vw, OFF_WT + b * 8192);
    ld4<0>(wv[4], sws, vw, OFF_WT + b * 8192 + 4096); ld4<256>(wv[5], sws, vw, OFF_WT + b * 8192 + 4096);
    ld4<512>(wv[6], sws, vw, OFF_WT + b * 8192 + 4096); ld4<768>(wv[7], sws, vw, OFF_WT + b * 8192 + 4096);
    __builtin_amdgcn_sched_barrier(0);
    float c1[8], s1[8], c2[8], s2[8];
#pragma unroll
    for (int i = 0; i < 8; ++i) {
      const float a = (float)((16 * (i >> 2) + 4 * g + (i & 3)) * (16 * w + n)) * (1.0f / 2048.0f);
      c1[i] = 16.0f * __builtin_amdgcn_cosf(a); s1[i] = 16.0f * __builtin_amdgcn_sinf(a);
      const float bb = (float)((16 * (i >> 2) + n) * (16 * w + 4 * g + (i & 3))) * (1.0f / 2048.0f);
      c2[i] = __builtin_amdgcn_cosf(bb); s2[i] = __builtin_amdgcn_sinf(bb);
    }
    pin(c1); pin(s1); pin(c2); pin(s2);
    if (has1) {
#pragma unroll
      for (int j = 0; j < NC1; ++j) {
        dma16(sw1, vb1, j * (16 * F_ * 2), lw + j * 2048);
        dma16(sw1, vb1, j * (16 * F_ * 2) + 8 * F_ * 2, lw + j * 2048 + 1024);
      }
      wait_vm<2 * NC1>();
    } else {
      wait_vm<0>();
    }
    pin(bc); pin(fb); pin(wv);
    __builtin_amdgcn_s_barrier();
    asm volatile("" ::: "memory");
    STAMP(1, 1);
#pragma unroll
    for (int i = 0; i < 4; ++i) {
      u4 u = __builtin_bit_cast(u4, bc[i]);
#pragma unroll
      for (int j = 0; j < 4; ++j) u[j] = __builtin_amdgcn_alignbit(u[j], u[j], 16) ^ 0x8000u;
      bs[i] = __builtin_bit_cast(h8, u);
    }
    f4 re[2], im[2];
    dft64(fft, n, g, bc, bs, re, im);
    float pr[8], pi[8];
#pragma unroll
    for (int i = 0; i < 8; ++i) {
      const float a = re[i >> 2][i & 3], bb = im[i >> 2][i & 3];
      pr[i] = a * c1[i] + bb * s1[i]; pi[i] = bb * c1[i] - a * s1[i];
    }
    h8 dr = mk_h8(pk_h(pr[0], pr[1]), pk_h(pr[2], pr[3]), pk_h(pr[4], pr[5]), pk_h(pr[6], pr[7]));
    h8 di = mk_h8(pk_h(pi[0], pi[1]), pk_h(pi[2], pi[3]), pk_h(pi[4], pi[5]), pk_h(pi[6], pi[7]));
#pragma unroll
    for (int t = 0; t < 2; ++t) {
      re[t] = MFMA16H(fb[3 * t + 1], di, MFMA16H(fb[3 * t], dr, z));
      im[t] = MFMA16H(fb[3 * t], di, MFMA16H(fb[3 * t + 2], dr, z));
    }
#pragma unroll
    for (int i = 0; i < 8; ++i) {
      const float a = re[i >> 2][i & 3], bb = im[i >> 2][i & 3], wr = bflo(wv[i]), wi = bfhi(wv[i]);
      pr[i] = a * wr - bb * wi; pi[i] = a * wi + bb * wr;
    }
    dr = mk_h8(pk_h(pr[0], pr[1]), pk_h(pr[2], pr[3]), pk_h(pr[4], pr[5]), pk_h(pr[6], pr[7]));
    di = mk_h8(pk_h(pi[0], pi[1]), pk_h(pi[2], pi[3]), pk_h(pi[4], pi[5]), pk_h(pi[6], pi[7]));
#pragma unroll
    for (int t = 0; t < 2; ++t) {
      re[t] = MFMA16H(di, fb[3 * t + 2], MFMA16H(dr, fb[3 * t], z));
      im[t] = MFMA16H(di, fb[3 * t], MFMA16H(dr, fb[3 * t + 1], z));
    }
#pragma unroll
    for (int t = 0; t < 2; ++t) {
      u4 o;
#pragma unroll
      for (int r = 0; r < 4; ++r) {
        const float a = re[t][r], bb = im[t][r], c = c2[4 * t + r], s = s2[4 * t + r];
        o[r] = pk_h(a * c - bb * s, -(a * s + bb * c));
      }
      *(u4*)(fft + 8192 + (16 * t + n) * 256 + (((4 * w + g) ^ n) << 4)) = o;
    }
    lds_barrier();
    dft64(fft + 8192, n, g, bc, bs, re, im);
#pragma unroll
    for (int t = 0; t < 2; ++t) {
      u4 o;
#pragma unroll
      for (int r = 0; r < 4; ++r) { const unsigned pk = pk_h(re[t][r], im[t][r]); o[r] = pk < 0xfffffffeu ? pk : 0xfffffffeu; }
      st16c(o, sws, OFF_Y1T + b * 8192 + (8 * (16 * w + n) + 4 * t + g) * 16);
    }
    STAMP(1, 2);
  } else if (rk > 28 || (xc & 1)) {
    const int pw = (rk > 28 ? (rk - 29) * 2 + (xc & 1) : 6) * 4 + w;
    unsigned acc = 0;
    constexpr int R2 = NC2 * 16;
    {
      const int pl = (lane & 7) < 7 ? (lane & 7) : 6;
      const unsigned char* pp = w1 + (xc >> 1) * 896 + pl * 128;
#pragma unroll
      for (int i = 0; i < (4 * R2 + 223) / 224; ++i) {
        int r = (i * 28 + pw) * 8 + (lane >> 3); r = r < 4 * R2 ? r : 4 * R2 - 1;
        acc ^= *(const unsigned*)(pp + (size_t)((r & 3) * 512 + NC1 * 16 + (r >> 2)) * (F_ * 2));
      }
    }
    {
      const unsigned char* pp = w2 + (xc >> 1) * 1024 + (lane & 7) * 128;
#pragma unroll
      for (int i = 0; i < 4; ++i) {
        const int r = (i * 28 + pw) * 8 + (lane >> 3);
        acc ^= *(const unsigned*)(pp + (size_t)((r & 3) * 448 + (r >> 2)) * (S_ * 2));
      }
    }
    asm volatile("" ::"v"(acc));
  }
  b8 B1[2 * (NC1 + NC2)];
  if (has1) {
    wait_vm<2>();
#pragma unroll
    for (int j = 0; j < NC1; ++j) { B1[2 * j] = trfrag(rb0 + j * 2048); B1[2 * j + 1] = trfrag(rb1 + j * 2048); }
  }

  {
    const int q = xc * 32 + rk;
    const u4 sx = make_srd(x, S_ * D_ * 2);
    const unsigned vx = (((g >> 1) * 224) + (g & 1) * 112 + n) * 4, sxo = (8 * q + 2 * w) * (D_ * 2);
    unsigned xv[7];
    ld4<0>(xv[0], sx, vx, sxo); ld4<64>(xv[1], sx, vx, sxo); ld4<128>(xv[2], sx, vx, sxo); ld4<192>(xv[3], sx, vx, sxo);
    ld4<256>(xv[4], sx, vx, sxo); ld4<320>(xv[5], sx, vx, sxo); ld4<384>(xv[6], sx, vx, sxo);
    h8 tb3[10];
#pragma unroll
    for (int i = 0; i < 10; ++i) ld16(tb3[i], stab, lane * 16, 22528 + i * 1024);
    if (has1) {
      __builtin_amdgcn_s_waitcnt(0xC07F);
#pragma unroll
      for (int j = 0; j < NC2; ++j) {
        dma16(sw1, vb1, (NC1 + j) * (16 * F_ * 2), lw + j * 2048);
        dma16(sw1, vb1, (NC1 + j) * (16 * F_ * 2) + 8 * F_ * 2, lw + j * 2048 + 1024);
      }
    }
    float tc[8], ts[8];
#pragma unroll
    for (int i = 0; i < 8; ++i) {
      const float a = (float)((2 * n + (i >> 2)) * (4 * (g & 1) + (i & 3))) * (1.0f / 448.0f);
      tc[i] = __builtin_amdgcn_cosf(a); ts[i] = __builtin_amdgcn_sinf(a);
    }
    const int ka = n & 7, rowb = (2 * (w & 1) + (n >> 3)) * 4, hsel = w >> 1;
    int ad[8];
#pragma unroll
    for (int i = 0; i < 8; ++i) {
      const int kf = ka + 14 * (4 * g + (i & 3));
      const int k2 = (i < 4) ? kf : 224 - kf;
      ad[i] = k2 * 32 + ((hsel ^ ((k2 >> 2) & 1)) << 4) + rowb;
    }
    pin(tc); pin(ts); pin(ad);
    {
      const int i0 = tid, i1 = tid < 194 ? tid + 256 : tid;
      const unsigned v0 = OFF_Y1T + (i0 >> 1) * 8192 + q * 32 + (i0 & 1) * 16;
      const unsigned v1 = OFF_Y1T + (i1 >> 1) * 8192 + q * 32 + (i1 & 1) * 16;
      u4 y0, y1;
      for (unsigned sp = 0; sp < SPIN_CAP; ++sp) {
        asm volatile("buffer_load_dwordx4 %0, %3, %2, 0 offen sc1\n\tbuffer_load_dwordx4 %1, %4, %2, 0 offen sc1\n\ts_waitcnt vmcnt(0)"
                     : "=&v"(y0), "=&v"(y1) : "s"(sws), "v"(v0), "v"(v1) : "memory");
        unsigned m = max(max(max(y0[0], y0[1]), max(y0[2], y0[3])), max(max(y1[0], y1[1]), max(y1[2], y1[3])));
        if (__builtin_amdgcn_ballot_w64(m == 0xffffffffu) == 0ull) break;
      }
      *(u4*)(fft + (i0 >> 1) * 32 + (((i0 & 1) ^ ((i0 >> 3) & 1)) << 4)) = y0;
      *(u4*)(fft + (i1 >> 1) * 32 + (((i1 & 1) ^ ((i1 >> 3) & 1)) << 4)) = y1;
    }
    pin(xv); pin(tb3);
    lds_barrier();
    STAMP(2, 1);
    unsigned dv[8];
#pragma unroll
    for (int i = 0; i < 8; ++i) dv[i] = *(const unsigned*)(fft + ad[i]);
    const h8 a0 = mk_h8(dv[0], dv[1], dv[2], dv[3]), a1 = mk_h8(dv[4], dv[5], dv[6], dv[7]);
    f4 y[2][2];
#pragma unroll
    for (int t = 0; t < 2; ++t) {
      const f4 vr = MFMA16H(a1, tb3[4 * t + 1], MFMA16H(a0, tb3[4 * t], z));
      const f4 vi = MFMA16H(a1, tb3[4 * t + 3], MFMA16H(a0, tb3[4 * t + 2], z));
      float pr[4], pi[4];
#pragma unroll
      for (int r = 0; r < 4; ++r) {
        const float c = tc[4 * t + r], s = ts[4 * t + r];
        pr[r] = vr[r] * c - vi[r] * s; pi[r] = vr[r] * s + vi[r] * c;
      }
      const h8 bb = mk_h8(pk_h(pr[0], pr[1]), pk_h(pr[2], pr[3]), pk_h(pi[0], pi[1]), pk_h(pi[2], pi[3]));
      y[t][0] = MFMA16H(tb3[8], bb, z); y[t][1] = MFMA16H(tb3[9], bb, z);
    }
    float v[14], sm = 0.f;
    unsigned* hst = (unsigned*)(lds + w * WREG + NC2 * 2048);
#pragma unroll
    for (int j = 0; j < 7; ++j) {
      const unsigned hp = pk_bf(bflo(xv[j]) + bfr(y[0][j >> 2][j & 3] * (1.0f / 224.0f)),
                                bfhi(xv[j]) + bfr(y[1][j >> 2][j & 3] * (1.0f / 224.0f)));
      hst[g * 112 + n + 16 * j] = hp;
      v[2 * j] = bflo(hp); v[2 * j + 1] = bfhi(hp); sm += v[2 * j] + v[2 * j + 1];
    }
    const float mean = sum32(sm) * (1.0f / 448.0f);
    float sq = 0.f;
#pragma unroll
    for (int j = 0; j < 14; ++j) { const float d = v[j] - mean; sq += d * d; }
    const float var = sum32(sq) * (1.0f / 448.0f);
    const float mb = bfr(mean);
    const float rs = 1.0f / bfr(sqrtf(bfr(bfr(var) + 1e-5f)));
    const float m2 = bfr((mean - mb) * rs);
    const float rs2 = 1.0f / bfr(sqrtf(bfr(bfr(var * rs * rs) + 1e-5f)));
    const float al = rs * rs2, be = -m2 * rs2;
    unsigned* pl = (unsigned*)(fft + 8192);
    const unsigned sel = lane < 32 ? 0x05040100u : 0x07060302u;
    unsigned* pw = pl + w * 448 + (g & 1) * 224 + 2 * n + hh;
#pragma unroll
    for (int j = 0; j < 7; ++j) {
      const unsigned pk = pk_bf((v[2 * j] - mb) * al + be, (v[2 * j + 1] - mb) * al + be);
      const auto r = __builtin_amdgcn_permlane32_swap(pk, pk, false, false);
      pw[32 * j] = __builtin_amdgcn_perm(r[1], r[0], sel);
    }
    lds_barrier();
    {
      const int d1 = tid < 192 ? tid + 256 : tid;
      const u4 o0 = {pl[tid], pl[448 + tid], pl[896 + tid], pl[1344 + tid]};
      const u4 o1 = {pl[d1], pl[448 + d1], pl[896 + d1], pl[1344 + d1]};
      st16c(o0, sws, OFF_HN + (q * 448 + tid) * 16);
      if (tid < 192) st16c(o1, sws, OFF_HN + (q * 448 + d1) * 16);
      const int c1 = lane < 48 ? lane + 64 : lane;
      const u4 h0 = ((const u4*)hst)[lane], h1 = ((const u4*)hst)[c1];
      st16c(h0, sws, OFF_H + sxo + lane * 16);
      if (lane < 48) st16c(h1, sws, OFF_H + sxo + c1 * 16);
    }
    STAMP(2, 2);
    if (has1) {
#pragma unroll
      for (int j = 0; j < NC2; ++j) { B1[2 * (NC1 + j)] = trfrag(rb0 + j * 2048); B1[2 * (NC1 + j) + 1] = trfrag(rb1 + j * 2048); }
    }
    wait_vm<0>();
    lds_barrier();
    if (tid == 0) __hip_atomic_store((unsigned*)(ws + OFF_FLG + q * 128), 1u, __ATOMIC_RELAXED, __HIP_MEMORY_SCOPE_AGENT);
    STAMP(2, 3);
  }
#pragma unroll
  for (int j = 0; j < NH2; ++j) {
    dma16(sw2, vb2, j * (16 * S_ * 2), lw + j * 2048);
    dma16(sw2, vb2, j * (16 * S_ * 2) + 8 * S_ * 2, lw + j * 2048 + 1024);
  }
  const int dl0 = wm * 32 + l31, dl1 = dl0 ^ 32;
  const unsigned va0 = dl0 < 56 ? (hh * D_ + dt * 56 + dl0) * 16 : 0x80000000u;
  const unsigned va1 = dl1 < 56 ? (hh * D_ + dt * 56 + dl1) * 16 : 0x80000000u;
  b8 fa[GW][2];
  float sum[16];

  if (has1) {
    constexpr int NJ = 32;
    const u4 sb1 = make_srd(b1, F_ * 2), sa = make_srd(ws + OFF_HN + w * (NJ * SA), NJ * SA);
    u2 ev[4];
    const unsigned vbi = (nt1 * 64 + wn * 32 + 4 * hh) * 2;
    ld8<0>(ev[0], sb1, vbi); ld8<16>(ev[1], sb1, vbi); ld8<32>(ev[2], sb1, vbi); ld8<48>(ev[3], sb1, vbi);
    poll_flag(sws, OFF_FLG + tid * 128);
    pin(ev);
    STAMP(3, 1);
#pragma unroll
    for (int j = 0; j < GW; ++j) { ld16c(fa[j][0], sa, va0, j * SA); ld16c(fa[j][1], sa, va1, j * SA); }
    f16v acc[4] = {};
#pragma unroll
    for (int j = 0; j < NJ; ++j) {
      const int sl = j % GW;
      wait_vm_n((NJ - 1 - j) < (GW - 1) ? (NJ - 1 - j) : (GW - 1), fa[sl][0], fa[sl][1]);
#pragma unroll
      for (int i = 0; i < 4; ++i) acc[i] = MFMA32B(B1[2 * j + (i >> 1)], fa[sl][i & 1], acc[i]);
      __builtin_amdgcn_sched_barrier(0);
      if (j + GW < NJ) { ld16c(fa[sl][0], sa, va0, (j + GW) * SA); ld16c(fa[sl][1], sa, va1, (j + GW) * SA); }
      __builtin_amdgcn_sched_barrier(0);
    }
    STAMP(3, 2);
    reduce_acc(lds, w, lane, acc, sum);
    const unsigned vo = dl0 < 56 ? (dt * 56 + dl0) * 16 + hh * 8 : 0x80000000u;
#pragma unroll
    for (int i = 0; i < 4; ++i) {
      float r[4];
#pragma unroll
      for (int j = 0; j < 4; ++j) {
        const unsigned rv = ev[i][j >> 1];
        const float u = bfr(bfr(sum[4 * i + j]) + ((j & 1) ? bfhi(rv) : bflo(rv)));
        const float zz = u * (1.0f + 0.044715f * u * u) * (-2.0f * 0.7978845608028654f * 1.4426950408889634f);
        r[j] = u * __builtin_amdgcn_rcpf(1.0f + __builtin_amdgcn_exp2f(zz));
      }
      const u2 o = {pk_bf(r[0], r[1]), pk_bf(r[2], r[3])};
      st8c<0>(o, sws, vo, OFF_UT + (nt1 * 8 + wn * 4 + i) * (D_ * 16));
    }
    STAMP(3, 3);
  } else {
    const int pw = ((rk - 28) * 2 + (xc & 1)) * 4 + w;
    const unsigned char* pp = w2 + (xc >> 1) * 1024 + (lane & 7) * 128;
    unsigned acc = 0;
#pragma unroll
    for (int i = 0; i < 4; ++i) {
      int r = (i * 32 + pw) * 8 + (lane >> 3); r = r < 896 ? r : 895;
      acc ^= *(const unsigned*)(pp + (size_t)((r & 3) * 448 + 224 + (r >> 2)) * (S_ * 2));
    }
    asm volatile("" ::"v"(acc));
    wait_vm<0>();
  }
  b8 B2[2 * NH2];
#pragma unroll
  for (int j = 0; j < NH2; ++j) { B2[2 * j] = trfrag(rb0 + j * 2048); B2[2 * j + 1] = trfrag(rb1 + j * 2048); }
  wait_vm<0>();
  lds_barrier();
  if (has1 && tid == 0)
    __hip_atomic_store((unsigned*)(ws + OFF_FLG + (256 + dt * 28 + nt1) * 128), 1u, __ATOMIC_RELAXED, __HIP_MEMORY_SCOPE_AGENT);
  STAMP(3, 4);
#pragma unroll
  for (int j = 0; j < NH2; ++j) {
    dma16(sw2, vb2, (NH2 + j) * (16 * S_ * 2), lw + j * 2048);
    dma16(sw2, vb2, (NH2 + j) * (16 * S_ * 2) + 8 * S_ * 2, lw + j * 2048 + 1024);
  }

  {
    constexpr int NJ = 28;
    const u4 sb2 = make_srd(b2, S_ * 2), sa = make_srd(ws + OFF_UT + w * (NJ * SA), NJ * SA);
    const int srow = nt2 * 64 + wn * 32 + l31;
    unsigned braw;
    ld4<0>(braw, sb2, (srow >> 1) * 4, 0);
    poll_flag(sws, OFF_FLG + (256 + dt * 28 + 7 * w + (lane < 7 ? lane : 0)) * 128);
    asm volatile("" : "+v"(braw));
    STAMP(4, 1);
    u2 ev[4];
    const unsigned eo = (srow * D_ + dt * 56 + wm * 32 + 4 * hh) * 2;
    ld8c<0>(ev[0], sws, OFF_H + eo); ld8c<16>(ev[1], sws, OFF_H + eo); ld8c<32>(ev[2], sws, OFF_H + eo); ld8c<48>(ev[3], sws, OFF_H + eo);
#pragma unroll
    for (int j = 0; j < GW; ++j) { ld16c(fa[j][0], sa, va0, j * SA); ld16c(fa[j][1], sa, va1, j * SA); }
    f16v acc[4] = {};
    b8 fb[2][2];
#pragma unroll
    for (int j = 0; j < NJ; ++j) {
      const int sl = j % GW;
      if (j + 1 >= NH2 && j + 1 < NJ) {
        fb[(j + 1) & 1][0] = trfrag(rb0 + (j + 1 - NH2) * 2048); fb[(j + 1) & 1][1] = trfrag(rb1 + (j + 1 - NH2) * 2048);
      }
      wait_vm_n((NJ - 1 - j) < (GW - 1) ? (NJ - 1 - j) : (GW - 1), fa[sl][0], fa[sl][1]);
#pragma unroll
      for (int i = 0; i < 4; ++i) acc[i] = MFMA32B(fa[sl][i & 1], j < NH2 ? B2[2 * j + (i >> 1)] : fb[j & 1][i >> 1], acc[i]);
      __builtin_amdgcn_sched_barrier(0);
      if (j + GW < NJ) { ld16c(fa[sl][0], sa, va0, (j + GW) * SA); ld16c(fa[sl][1], sa, va1, (j + GW) * SA); }
      __builtin_amdgcn_sched_barrier(0);
    }
    STAMP(4, 2);
    pin(ev);
    reduce_acc(lds, w, lane, acc, sum);
    const float bv = (srow & 1) ? bfhi(braw) : bflo(braw);
    u2* op = out + eo / 8;
#pragma unroll
    for (int i = 0; i < 4; ++i) {
      float r[4];
#pragma unroll
      for (int j = 0; j < 4; ++j) {
        const unsigned rv = ev[i][j >> 1];
        r[j] = bfr(bfr(sum[4 * i + j]) + bv) + ((j & 1) ? bfhi(rv) : bflo(rv));
      }
      const u2 o = {pk_bf(r[0], r[1]), pk_bf(r[2], r[3])};
      if (i < 3 || wm == 0) op[2 * i] = o;
    }
    STAMP_END(4, 3);
  }
}

extern "C" void kernel_launch(void* const* d_in, const int* in_sizes, int n_in, void* d_out, int out_size, void* d_ws,
                              size_t ws_size, hipStream_t stream) {
  typedef const unsigned char* cp;
  k1_rowfft<<<256, 512, 0, stream>>>((const unsigned*)d_in[0], (const u4*)d_in[1], (const u4*)d_in[2],
                                     (unsigned char*)d_ws, (cp)d_in[4]);
  k2_mega<<<256, 256, 0, stream>>>((unsigned char*)d_ws, (cp)d_in[0], (cp)d_in[4], (cp)d_in[5], (cp)d_in[6], (cp)d_in[7],
                                   (u2*)d_out);
}
```

```cpp
#include <hip/hip_runtime.h>
#include <stdint.h>

typedef _Float16 h2 __attribute__((ext_vector_type(2)));
typedef __bf16 b8 __attribute__((ext_vector_type(8)));
typedef __bf16 b2 __attribute__((ext_vector_type(2)));
typedef short s4 __attribute__((ext_vector_type(4)));
typedef float f2 __attribute__((ext_vector_type(2)));
typedef float f4 __attribute__((ext_vector_type(4)));
typedef unsigned u2 __attribute__((ext_vector_type(2)));
typedef unsigned u4 __attribute__((ext_vector_type(4)));
#define DI __device__ __forceinline__
#define LDSP __attribute__((address_space(3)))

constexpr int S_ = 2048, D_ = 448, DF_ = 225, F_ = 1792;

namespace tb {
constexpr double PI = 3.14159265358979323846264338327950288;
constexpr double cos_rev(int num, int den) {
  num %= den; if (num < 0) num += den;
  if (2 * num > den) num = den - num;
  if (4 * num == den) return 0.0;
  double sg = 1.0, a = 2.0 * PI * num / den;
  if (4 * num > den) { sg = -1.0; a = PI * (den - 2 * num) / den; }
  double t = 1.0, s = 1.0, a2 = a * a;
  for (int i = 1; i <= 13; ++i) { t = -t * a2 / ((2 * i - 1) * (2 * i)); s += t; }
  return sg * s;
}
constexpr unsigned short f2h(double v) {
  if (v == 0.0) return 0;
  unsigned sg = v < 0 ? 0x8000u : 0u; if (v < 0) v = -v;
  int e = 0; double p = 1.0;
  while (v >= 2.0 * p) { p *= 2.0; ++e; }
  while (v < p) { p *= 0.5; --e; }
  if (e < -14) { e = -14; p = 1.0 / 16384.0; }
  double q = v / p * 1024.0;
  long qi = (long)q; double fr = q - (double)qi;
  if (fr > 0.5 || (fr == 0.5 && (qi & 1))) ++qi;
  long bits = qi < 1024 ? qi : (long)((e + 15) << 10) + (qi - 1024);
  return (unsigned short)(sg | (unsigned)bits);
}
struct Base { unsigned short c1[448], cq[448], c2[448], c8[64]; };
constexpr Base make_base() {
  Base b{};
  for (int i = 0; i < 448; ++i) { double c = cos_rev(i, 448); b.c1[i] = f2h(c); b.cq[i] = f2h(0.25 * c); b.c2[i] = f2h(2.0 * c); }
  for (int i = 0; i < 64; ++i) b.c8[i] = f2h(0.125 * cos_rev(i, 64));
  return b;
}
constexpr Base BASE = make_base();
constexpr int m448(int m) { return ((m % 448) + 448) % 448; }
constexpr unsigned short neg(unsigned short v) { return (unsigned short)(v ^ 0x8000u); }
struct T1 { unsigned short v[8][64][8]; };
constexpr T1 make_t1() {
  T1 t{};
  for (int l = 0; l < 64; ++l) for (int j = 0; j < 8; ++j) {
    const int n = l & 15, g = l >> 4;
    {
      const int r = n >> 3, ka = n & 7, rp = g >> 1, d1 = 7 * (g & 1) + j;
      const bool on = (r == rp) && j < 7;
      t.v[0][l][j] = on ? BASE.c1[m448(32 * d1 * ka)] : 0;
      t.v[1][l][j] = on ? neg(BASE.c1[m448(32 * d1 * ka - 112)]) : 0;
    }
    for (int tt = 0; tt < 2; ++tt) {
      const int kb = 16 * tt + n, d2 = 8 * g + j;
      const unsigned short c = BASE.cq[m448(14 * d2 * kb)], s = BASE.cq[m448(14 * d2 * kb - 112)];
      t.v[2 + 3 * tt][l][j] = c; t.v[3 + 3 * tt][l][j] = s; t.v[4 + 3 * tt][l][j] = tt ? neg(c) : neg(s);
    }
  }
  return t;
}
struct T2 { unsigned short a[4][4][64][8]; };
constexpr T2 make_t2() {
  T2 t{};
  for (int w = 0; w < 4; ++w) for (int ks = 0; ks < 4; ++ks) for (int l = 0; l < 64; ++l) for (int jj = 0; jj < 4; ++jj) {
    const int k1a = 16 * w + (l & 15), s1 = 16 * ks + 4 * (l >> 4) + jj, m = s1 * k1a;
    t.a[w][ks][l][2 * jj] = BASE.c8[m & 63];
    t.a[w][ks][l][2 * jj + 1] = BASE.c8[(m - 16) & 63];
  }
  return t;
}
struct T2b { unsigned short b[2][3][64][8]; };
constexpr T2b make_t2b() {
  T2b t{};
  for (int tt = 0; tt < 2; ++tt) for (int l = 0; l < 64; ++l) for (int j = 0; j < 8; ++j) {
    const int kk = 16 * tt + (l & 15), ss = 16 * (j >> 2) + 4 * (l >> 4) + (j & 3);
    const unsigned short c = BASE.cq[m448(14 * kk * ss)], s = BASE.cq[m448(14 * kk * ss - 112)];
    t.b[tt][0][l][j] = c; t.b[tt][1][l][j] = s; t.b[tt][2][l][j] = neg(s);
  }
  return t;
}
struct T3 { unsigned short v[10][64][8]; };
constexpr T3 make_t3() {
  T3 t{};
  for (int l = 0; l < 64; ++l) for (int j = 0; j < 8; ++j) {
    const int n = l & 15, g = l >> 4;
    for (int tt = 0; tt < 2; ++tt) for (int ks = 0; ks < 2; ++ks) {
      const int kb = 16 * ks + 4 * g + (j >> 1), part = j & 1, d2 = 2 * n + tt;
      const unsigned short c = BASE.cq[m448(14 * d2 * kb)], s = BASE.cq[m448(14 * d2 * kb - 112)];
      t.v[(tt * 2 + 0) * 2 + ks][l][j] = part == 0 ? c : (ks == 0 ? s : neg(s));
      t.v[(tt * 2 + 1) * 2 + ks][l][j] = part == 0 ? s : (ks == 0 ? neg(c) : c);
    }
    for (int u = 0; u < 2; ++u) {
      const int gp = n >> 2, rg = n & 3, e = 4 * u + rg, d1 = 7 * (gp & 1) + e;
      const int ka = 4 * (g & 1) + (j & 3), part = j >> 2;
      const bool on = ((gp >> 1) == (g >> 1)) && e < 7;
      const bool one = (ka == 0 || ka == 7);
      const int idx = m448(32 * d1 * ka - (part ? 112 : 0));
      unsigned short val = one ? BASE.c1[idx] : BASE.c2[idx];
      if (part) val = neg(val);
      t.v[8 + u][l][j] = on ? val : 0;
    }
  }
  return t;
}
}
struct TabM { tb::T2 a; tb::T2b b; tb::T3 c; tb::T1 d; };
__device__ __attribute__((aligned(16))) const TabM TABM = {tb::make_t2(), tb::make_t2b(), tb::make_t3(), tb::make_t1()};

DI float bfr(float x) { return (float)(__bf16)x; }
DI float bflo(unsigned v) { return __uint_as_float(v << 16); }
DI float bfhi(unsigned v) { return __uint_as_float(v & 0xffff0000u); }
DI unsigned pk_bf(float a, float b) { f2 v = {a, b}; return __builtin_bit_cast(unsigned, __builtin_convertvector(v, b2)); }
DI unsigned pk_h(float a, float b) { f2 v = {a, b}; return __builtin_bit_cast(unsigned, __builtin_convertvector(v, h2)); }
template <int C> DI float dpp_add(float v) {
  return v + __uint_as_float((unsigned)__builtin_amdgcn_update_dpp(0, (int)__float_as_uint(v), C, 0xf, 0xf, false));
}
DI float sum32(float v) {
  v = dpp_add<0x128>(v); v = dpp_add<0x124>(v); v = dpp_add<0x122>(v); v = dpp_add<0x121>(v);
  auto r = __builtin_amdgcn_permlane16_swap(__float_as_uint(v), __float_as_uint(v), false, false);
  return __uint_as_float(r[0]) + __uint_as_float(r[1]);
}
DI void row_stats(const float (&v)[14], int lane, float& mean, float& var) {
  const float p0 = __uint_as_float(__builtin_amdgcn_readlane(__float_as_uint(v[0]), 0));
  const float p1 = __uint_as_float(__builtin_amdgcn_readlane(__float_as_uint(v[0]), 32));
  const float p = lane < 32 ? p0 : p1;
  f2 sa = {0.f, 0.f}, sb = {0.f, 0.f};
#pragma unroll
  for (int j = 0; j < 7; ++j) { const f2 d = {v[2 * j] - p, v[2 * j + 1] - p}; sa += d; sb += d * d; }
  const float m = sum32(sa[0] + sa[1]) * (1.0f / 448.0f), e = sum32(sb[0] + sb[1]) * (1.0f / 448.0f);
  mean = p + m;
  var = fmaxf(e - m * m, 0.f);
}
DI float ln_rstd(float var) { return __builtin_amdgcn_rcpf(bfr(__builtin_amdgcn_sqrtf(bfr(bfr(var) + 1e-5f)))); }
template <typename T, int N> DI void pin(T (&a)[N]) {
#pragma unroll
  for (int i = 0; i < N; ++i) asm volatile("" : "+v"(a[i]));
}
#define M16 "v_mfma_f32_16x16x32_f16 "
struct Dma { u4 srd; unsigned v[4]; unsigned lds; };
#define DM0(l, i) "s_add_u32 m0, " l ", " i "\n\ts_nop 0\n\t"
#define DLD(v, r, so, off) "buffer_load_dwordx4 " v ", " r ", " so " offen offset:" off " lds\n\t"
#define O4(o) "=&v"(o[0]), "=&v"(o[1]), "=&v"(o[2]), "=&v"(o[3])
#define DIN(d, so, SL) "s"(d.srd), "v"(d.v[0]), "v"(d.v[1]), "v"(d.v[2]), "v"(d.v[3]), "s"(d.lds), "s"(so), "n"((SL) * 4096)
DI void mfma_2x2(f4 (&o)[4], const u4& a0, const u4& a1, const u4& b0, const u4& b1) {
  asm("s_nop 1\n\t" M16 "%0, %4, %6, 0\n\t" M16 "%1, %4, %7, 0\n\t" M16 "%2, %5, %6, 0\n\t" M16 "%3, %5, %7, 0\n\ts_nop 7"
      : O4(o) : "v"(a0), "v"(a1), "v"(b0), "v"(b1));
}
template <int SL> DI void mfma_2x2_d(f4 (&o)[4], const u4& a0, const u4& a1, const u4& b0, const u4& b1, const Dma& d, unsigned so) {
  asm volatile(DM0("%13", "%15") M16 "%0, %4, %6, 0\n\t" DLD("%9", "%8", "%14", "0") M16 "%1, %4, %7, 0\n\t" DLD("%10", "%8", "%14", "1024")
               M16 "%2, %5, %6, 0\n\t" DLD("%11", "%8", "%14", "2048") M16 "%3, %5, %7, 0\n\t" DLD("%12", "%8", "%14", "3072") "s_nop 6"
               : O4(o) : "v"(a0), "v"(a1), "v"(b0), "v"(b1), DIN(d, so, SL) : "memory", "scc");
}
DI void mfma_pq(f4 (&o)[4], const u4& p, const u4& q, const u4& t0, const u4& t1, const u4& t2, const u4& t3,
                const u4& t4, const u4& t5, const u4& t6, const u4& t7) {
  asm("s_nop 1\n\t" M16 "%0, %6, %4, 0\n\t" M16 "%1, %8, %4, 0\n\t" M16 "%2, %10, %4, 0\n\t" M16 "%3, %12, %4, 0\n\t"
      M16 "%0, %7, %5, %0\n\t" M16 "%1, %9, %5, %1\n\t" M16 "%2, %11, %5, %2\n\t" M16 "%3, %13, %5, %3\n\ts_nop 7"
      : O4(o) : "v"(p), "v"(q), "v"(t0), "v"(t1), "v"(t2), "v"(t3), "v"(t4), "v"(t5), "v"(t6), "v"(t7));
}
#define PQ_D(X) DM0("%19", "%21") M16 "%0, " X("%6", "%4") ", 0\n\t" M16 "%1, " X("%8", "%4") ", 0\n\t" DLD("%15", "%14", "%20", "0") \
    M16 "%2, " X("%10", "%4") ", 0\n\t" M16 "%3, " X("%12", "%4") ", 0\n\t" DLD("%16", "%14", "%20", "1024")                          \
    M16 "%0, " X("%7", "%5") ", %0\n\t" M16 "%1, " X("%9", "%5") ", %1\n\t" DLD("%17", "%14", "%20", "2048")                           \
    M16 "%2, " X("%11", "%5") ", %2\n\t" M16 "%3, " X("%13", "%5") ", %3\n\t" DLD("%18", "%14", "%20", "3072") "s_nop 6"
#define XAB(t, d) t ", " d
#define XBA(t, d) d ", " t
template <bool TA, int SL> DI void mfma_pq_d(f4 (&o)[4], const u4& p, const u4& q, const u4& t0, const u4& t1, const u4& t2, const u4& t3,
                                             const u4& t4, const u4& t5, const u4& t6, const u4& t7, const Dma& d, unsigned so) {
  if constexpr (TA)
    asm volatile(PQ_D(XAB) : O4(o) : "v"(p), "v"(q), "v"(t0), "v"(t1), "v"(t2), "v"(t3), "v"(t4), "v"(t5), "v"(t6), "v"(t7), DIN(d, so, SL) : "memory", "scc");
  else
    asm volatile(PQ_D(XBA) : O4(o) : "v"(p), "v"(q), "v"(t0), "v"(t1), "v"(t2), "v"(t3), "v"(t4), "v"(t5), "v"(t6), "v"(t7), DIN(d, so, SL) : "memory", "scc");
}
#define L4(X, c, s, d0, d1, Z0, Z1, Z2, Z3, LA, LB) M16 "%0, " X(c, d0) ", " Z0 "\n\t" M16 "%1, " X(s, d0) ", " Z1 "\n\t" LA \
    M16 "%2, " X(c, d1) ", " Z2 "\n\t" M16 "%3, " X(s, d1) ", " Z3 "\n\t" LB
#define D64(X) DM0("%25", "%27")                                                                          \
    L4(X, "%12", "%16", "%4", "%8", "0", "0", "0", "0", DLD("%21", "%20", "%26", "0"), "")                  \
    L4(X, "%13", "%17", "%5", "%9", "%0", "%1", "%2", "%3", DLD("%22", "%20", "%26", "1024"), "")           \
    L4(X, "%14", "%18", "%6", "%10", "%0", "%1", "%2", "%3", DLD("%23", "%20", "%26", "2048"), "")          \
    L4(X, "%15", "%19", "%7", "%11", "%0", "%1", "%2", "%3", DLD("%24", "%20", "%26", "3072"), "") "s_nop 7"
template <bool TA, int SL> DI void mfma_dft64_d(f4 (&o)[4], const u4 (&e)[8], const u4 (&c)[4], const u4 (&s)[4], const Dma& d, unsigned so) {
#define D64IN "v"(e[0]), "v"(e[1]), "v"(e[2]), "v"(e[3]), "v"(e[4]), "v"(e[5]), "v"(e[6]), "v"(e[7]), "v"(c[0]), "v"(c[1]), "v"(c[2]), \
              "v"(c[3]), "v"(s[0]), "v"(s[1]), "v"(s[2]), "v"(s[3]), DIN(d, so, SL)
  if constexpr (TA) asm volatile(D64(XAB) : O4(o) : D64IN : "memory", "scc");
  else asm volatile(D64(XBA) : O4(o) : D64IN : "memory", "scc");
}
DI u4 pk4h(const f4& a, const f4& b) { u4 r = {pk_h(a[0], a[1]), pk_h(a[2], a[3]), pk_h(b[0], b[1]), pk_h(b[2], b[3])}; return r; }
DI u4 zip4h(const f4& a, const f4& b) { u4 r = {pk_h(a[0], b[0]), pk_h(a[1], b[1]), pk_h(a[2], b[2]), pk_h(a[3], b[3])}; return r; }
DI void lds_barrier() {
  asm volatile("" ::: "memory");
  __builtin_amdgcn_s_waitcnt(0xC07F);
  __builtin_amdgcn_s_barrier();
  asm volatile("" ::: "memory");
}

constexpr unsigned PXW = 2 * 7296 + 128, PY = 8192 + 384;
constexpr unsigned OFF_XW = 0, OFF_Y = 4u << 20, OFF_HN = 6u << 20, OFF_UT = 8u << 20, OFF_H = 10u << 20,
                   OFF_FLG = 12u << 20, OFF_EPOCH = OFF_FLG + 1000 * 1024, WS_BYTES = 13u << 20;
constexpr int NREP = 8;
constexpr unsigned FREP = 772 * 128, FS = 64, FL1 = 16384, FLUA = 32768, FLUB = 65536;
constexpr unsigned CODEW = 25088;
constexpr unsigned OOB = 0x80000000u;

DI u4 trfrag(const unsigned char* p) {
  const s4 lo = __builtin_amdgcn_ds_read_tr16_b64_v4i16((LDSP s4*)p);
  const s4 hi = __builtin_amdgcn_ds_read_tr16_b64_v4i16((LDSP s4*)(p + 512));
  return __builtin_bit_cast(u4, __builtin_shufflevector(lo, hi, 0, 1, 2, 3, 4, 5, 6, 7));
}
DI u4 make_srd(const void* p, unsigned bytes) {
  const unsigned long long a = (unsigned long long)p;
  u4 r = {(unsigned)a, (unsigned)(a >> 32) & 0xffffu, bytes, 0x00020000u};
  return r;
}
template <int SLOT> DI void dma2(const u4& srd, const unsigned (&v)[4], unsigned soff, unsigned ldsbase) {
  asm volatile("s_add_u32 m0, %6, %7\n\ts_nop 0\n\t"
               "buffer_load_dwordx4 %1, %0, %5 offen lds\n\t"
               "buffer_load_dwordx4 %2, %0, %5 offen offset:1024 lds\n\t"
               "buffer_load_dwordx4 %3, %0, %5 offen offset:2048 lds\n\t"
               "buffer_load_dwordx4 %4, %0, %5 offen offset:3072 lds"
               ::"s"(srd), "v"(v[0]), "v"(v[1]), "v"(v[2]), "v"(v[3]), "s"(soff), "s"(ldsbase), "n"(SLOT * 4096) : "memory", "scc");
}
template <int SLOT, int K> DI void dma1(const u4& srd, const unsigned (&v)[4], unsigned soff, unsigned ldsbase) {
  asm volatile("s_add_u32 m0, %3, %4\n\ts_nop 0\n\tbuffer_load_dwordx4 %1, %0, %2 offen offset:%5 lds"
               ::"s"(srd), "v"(v[K]), "s"(soff), "s"(ldsbase), "n"(SLOT * 4096), "n"(K * 1024) : "memory", "scc");
  __builtin_amdgcn_sched_barrier(0);
}
template <typename T> DI void ld16(T& dst, const u4& srd, unsigned voff, unsigned soff) {
  asm volatile("buffer_load_dwordx4 %0, %2, %1, %3 offen" : "=v"(dst) : "s"(srd), "v"(voff), "s"(soff) : "memory");
}
template <int OFF = 0, typename T> DI void ld16c(T& dst, const u4& srd, unsigned voff, unsigned soff) {
  asm volatile("buffer_load_dwordx4 %0, %2, %1, %3 offen offset:%4 sc1" : "=v"(dst) : "s"(srd), "v"(voff), "s"(soff), "n"(OFF) : "memory");
}
template <int OFF> DI void ld8(u2& dst, const u4& srd, unsigned voff) {
  asm volatile("buffer_load_dwordx2 %0, %2, %1, 0 offen offset:%3" : "=v"(dst) : "s"(srd), "v"(voff), "n"(OFF) : "memory");
}
template <int OFF> DI void ld8c(u2& dst, const u4& srd, unsigned voff) {
  asm volatile("buffer_load_dwordx2 %0, %2, %1, 0 offen offset:%3 sc1" : "=v"(dst) : "s"(srd), "v"(voff), "n"(OFF) : "memory");
}
template <int OFF> DI void ld4(unsigned& dst, const u4& srd, unsigned voff, unsigned soff) {
  asm volatile("buffer_load_dword %0, %2, %1, %3 offen offset:%4" : "=v"(dst) : "s"(srd), "v"(voff), "s"(soff), "n"(OFF) : "memory");
}
DI void touch(unsigned& dst, const u4& srd, unsigned voff) { ld4<0>(dst, srd, voff, 0); }
template <int OFF = 0> DI void st16c(const u4& v, const u4& srd, unsigned voff, unsigned soff = 0) {
  asm volatile("buffer_store_dwordx4 %0, %2, %1, %3 offen offset:%4 sc1\n\ts_nop 1" ::"v"(v), "s"(srd), "v"(voff), "s"(soff), "n"(OFF) : "memory");
}
DI void st8c(const u2& v, const u4& srd, unsigned voff, unsigned soff) {
  asm volatile("buffer_store_dwordx2 %0, %2, %1, %3 offen sc1" ::"v"(v), "s"(srd), "v"(voff), "s"(soff) : "memory");
}
template <int N> DI void wait_vm() { asm volatile("s_waitcnt vmcnt(%0)" ::"n"(N) : "memory"); }
template <int N, typename T> DI void wait_vm(T& a) { asm volatile("s_waitcnt vmcnt(%1)" : "+v"(a) : "n"(N) : "memory"); }
template <int I> struct IC { static constexpr int v = I; };
template <int I, int N, typename F> DI void static_for(F&& f) {
  if constexpr (I < N) { f(IC<I>{}); static_for<I + 1, N>(f); }
}
DI void wait_lgkm8() { asm volatile("" ::: "memory"); __builtin_amdgcn_s_waitcnt(0xC87F); asm volatile("" ::: "memory"); }
DI void wait_lgkm() { asm volatile("" ::: "memory"); __builtin_amdgcn_s_waitcnt(0xC07F); asm volatile("" ::: "memory"); }
constexpr unsigned SPIN_CAP = 1u << 20;
DI void poll_flag(const u4& srd, unsigned voff, u2 tag) {
#pragma unroll 1
  for (unsigned sp = 0; sp < SPIN_CAP; ++sp) {
    u2 f;
    asm volatile("buffer_load_dwordx2 %0, %2, %1, 0 offen sc1\n\ts_waitcnt vmcnt(0)" : "=v"(f) : "s"(srd), "v"(voff) : "memory");
    if (f[0] == tag[0] && f[1] == tag[1]) voff = OOB;
    if (__builtin_amdgcn_ballot_w64(voff != OOB) == 0ull) break;
    __builtin_amdgcn_s_sleep(2);
  }
}
#define MFMA16B(a, b, c) __builtin_amdgcn_mfma_f32_16x16x32_bf16(__builtin_bit_cast(b8, (a)), __builtin_bit_cast(b8, (b)), (c), 0, 0, 0)
#define SB() __builtin_amdgcn_sched_barrier(0)
constexpr int GL = 16;
constexpr int WREG = 8 * 4096;
constexpr int GEN = 4 * WREG;
constexpr unsigned SA = 4 * D_ * 16;

DI unsigned red_off(int buf, int dst, int c) {
  return buf == 0 ? GEN + (dst * 3 + c - 1) * 2048 : (c < 3 ? dst * WREG + 7 * 4096 + (c - 1) * 2048 : GEN + 24576 + dst * 2048);
}
DI unsigned gelu_pk(float a, float b, unsigned bias) {
  const unsigned sp = pk_bf(a, b);
  const unsigned up = pk_bf(bflo(sp) + bflo(bias), bfhi(sp) + bfhi(bias));
  const f2 u = {bflo(up), bfhi(up)};
  constexpr float K0 = -2.0f * 0.7978845608028654f * 1.4426950408889634f;
  const f2 zz = u * ((u * u) * (0.044715f * K0) + K0);
  const f2 rc = {__builtin_amdgcn_rcpf(1.0f + __builtin_amdgcn_exp2f(zz[0])), __builtin_amdgcn_rcpf(1.0f + __builtin_amdgcn_exp2f(zz[1]))};
  const f2 r = u * rc;
  return pk_bf(r[0], r[1]);
}
struct S1 {
  static constexpr int N = 64, TS = 45, TF = 56;
  static constexpr int issue(int u) { return u < GL ? -1 : u - GL + 1; }
  static constexpr int extra(int t) { return (t == TS ? 1 : 0) + (t == TF ? 1 : 0); }
};
struct S2 {
  static constexpr int N = 56, TG = 16, TP = TG - 7, TH = 30;
  static constexpr int issue(int u) {
    if (u < GL) return -1;
    const int a = u - GL + 1, c = TG + (u - 28) / 2;
    return (u < 28 || a > c) ? a : c;
  }
  static constexpr int extra(int t) { return (t == TP ? 1 : 0) + (t == TH ? 2 : 0); }
};
template <typename S> constexpr int pend_load(int u, int t) {
  int c = 0;
  for (int v = u + 1; v < S::N; ++v) if (S::issue(v) <= t) ++c;
  for (int k = S::issue(u) < 0 ? 0 : S::issue(u); k <= t; ++k) c += S::extra(k);
  return c;
}
template <typename S> constexpr int pend_extra(int ts, int last_load, int last_extra) {
  int c = 0;
  for (int v = 0; v < S::N; ++v) if (S::issue(v) > ts && S::issue(v) <= last_load) ++c;
  for (int k = ts + 1; k <= last_extra; ++k) c += S::extra(k);
  return c;
}
constexpr int b2_slot(int p) { return p < 2 ? p + 5 : p < 4 ? p + 1 : p < 7 ? p - 4 : p < 9 ? p - 2 : p < 11 ? p - 6 : p - 11; }

__global__ __launch_bounds__(256, 1) void k_mega(unsigned char* ws, const unsigned char* x, const unsigned char* wr,
                                                 const unsigned char* wi, const unsigned char* w1, const unsigned char* b1,
                                                 const unsigned char* w2, const unsigned char* b2, unsigned char* out) {
  __shared__ __attribute__((aligned(16))) unsigned char lds[163840];
  const int b = blockIdx.x, tid = threadIdx.x;
  const int w = __builtin_amdgcn_readfirstlane(tid >> 6), lane = tid & 63, n = lane & 15, g = lane >> 4;
  const int xc = b & 7, rk = b >> 3, q = xc * 32 + rk;
  const bool has1 = rk < 25 - (xc & 1), has2 = rk < 28;
  const unsigned fbase = OFF_FLG + (xc % NREP) * FREP;
  const int col = has2 ? xc * 28 + rk : 224;
  const int L1 = (xc >> 1) * 49 + (xc & 1) * 25 + rk, nt1 = L1 / 7, dt1 = L1 - 7 * nt1, nt2 = 4 * xc + rk / 7, dt2 = rk % 7;
  const u4 sws = make_srd(ws, WS_BYTES), stab = make_srd(&TABM, sizeof(TabM)), sx = make_srd(x, S_ * D_ * 2);
  unsigned char* G0 = lds + GEN;
  unsigned char* G1 = G0 + 8192;
  const int wn = w >> 1, hh = lane >> 5, r1 = xc, r2 = (7 * xc) >> 2;
  u2 tag, ev;
  unsigned pf[2], pg[2], braw, pft, pfc;
  const u4 sp2 = make_srd(w2, w == 3 ? F_ * S_ * 2 : 0);
  const u4 sp1 = make_srd(w1, w == 3 ? S_ * F_ * 2 : 0);
  unsigned vp1;
  f4 c1[2], s1[2], c2[2], s2[2], tc3[2], ts3[2];
  int la[2], ad[8];
  unsigned vb1[4], vb2[4], xv[7], vc, vf, vfl;
  u4 bc[4], tb3[10];
  int lr[4], lu, rbo[4];
  unsigned vy, vp2, vp3, lwv;
  const unsigned sxo = (32 * (8 * (q & 7) + 2 * w) + (q >> 3)) * (D_ * 2);
  const unsigned lw = (unsigned)(size_t)(LDSP unsigned char*)lds + w * WREG;
  const u4 sw1 = make_srd(w1 + w * (8 * F_ * 2), has1 ? (S_ - 24) * F_ * 2 : 0), sw2 = make_srd(w2 + w * (448 * S_ * 2), has2 ? 448 * S_ * 2 : 0);
#define ROT1(p) (((p) + 2 * r1) & 15)
#define ROT2(p) ((p) + r2 < 14 ? (p) + r2 : (p) + r2 - 14)
#define SO1(p) ((1024 * ((p) & 1) + ((((p) >> 1) + r1) & 7)) * (F_ * 2))
#define DMA1(p, s) dma2<s>(sw1, vb1, SO1(p), lw)
#define DMA2(p, s) dma2<s>(sw2, vb2, ROT2(p) * (32 * S_ * 2), lw)
#define DP1(p, s, k) dma1<(s), (k)>(sw1, vb1, SO1(p), lw)
#define DP2(p, s, k) dma1<(s), (k)>(sw2, vb2, ROT2(p) * (32 * S_ * 2), lw)

  {
    const unsigned vx = ((g >> 1) * (32 * 224) + (g & 1) * 112 + n) * 4;
    ld4<0>(xv[0], sx, vx, sxo); ld4<64>(xv[1], sx, vx, sxo); ld4<128>(xv[2], sx, vx, sxo); ld4<192>(xv[3], sx, vx, sxo);
    ld4<256>(xv[4], sx, vx, sxo); ld4<320>(xv[5], sx, vx, sxo); ld4<384>(xv[6], sx, vx, sxo);
    u4 tb1[8];
#pragma unroll
    for (int i = 0; i < 8; ++i) ld16(tb1[i], stab, lane * 16, 32768 + i * 1024);
    ld8c<0>(tag, sws, OFF_EPOCH);
    u4 wa, wb;
    {
      const unsigned vw = tid < 225 ? tid * 16 : OOB;
      ld16(wa, make_srd(wr, S_ * DF_ * 2), vw, q * 3600); ld16(wb, make_srd(wi, S_ * DF_ * 2), vw, q * 3600);
      const u4 sb1 = make_srd(b1, F_ * 2), sb2 = make_srd(b2, S_ * 2);
      ld8<0>(ev, sb1, (nt1 * 64 + w * 16 + 4 * g) * 2);
      ld4<0>(braw, sb2, (nt2 * 32 + w * 8 + (n >> 1)) * 4, 0);
      ld4<0>(pft, stab, tid < 8 ? (rk * 8 + tid) * 128 : OOB, 0);
      ld4<0>(pfc, make_srd((const void*)(__builtin_amdgcn_s_getpc() & ~127ull), CODEW), tid < 8 ? (rk * 8 + tid) * 128 : OOB, 0);
    }
    SB();
    const int ka = n & 7;
    f4 tc[2], ts[2];
#pragma unroll
    for (int i = 0; i < 8; ++i) {
      const float a = (float)((8 * g + i) * ka) * (1.0f / 448.0f);
      tc[i & 1][i >> 1] = 0.25f * __builtin_amdgcn_cosf(a); ts[i & 1][i >> 1] = 0.25f * __builtin_amdgcn_sinf(a);
    }
    int lo0 = (w >> 1) * 3712 + (2 * (w & 1) + (n >> 3)) * 4 + (ka + 56 * g) * 16, lo1 = lo0 + (224 - 42 - 2 * (ka + 56 * g)) * 16;
    int ls = (tid & 1) * 3712 + (tid >> 1) * 16, lg = 8192 + ((tid & 1) * 900 + (tid >> 1)) * 2;
    unsigned vo = tid < 192 ? OFF_XW + q * PXW + tid * 16 : OOB, vo2 = tid < 66 ? vo + 2 * 3072 : OOB, vs[3];
#pragma unroll
    for (int i = 0; i < 3; ++i) {
      const int c = b * 450 + i * 192 + tid;
      vs[i] = (tid < 192 && i * 192 + tid < 450) ? OFF_Y + (c >> 9) * PY + (c & 511) * 16 : OOB;
    }
    pin(vs);
    asm volatile("" : "+v"(lo0), "+v"(lo1), "+v"(ls), "+v"(lg), "+v"(vo), "+v"(vo2));
    {
      const int pr = rk * 16 + (lane >> 2);
      vp1 = (pr >> 1) * 8 * (F_ * 2) + ((7 * xc) >> 1) * 128 + (lane & 3) * 128;
    }
    pin(tc); pin(ts);
    asm volatile("" : "+v"(vp1));
    {
      const u4 ones = {~0u, ~0u, ~0u, ~0u};
      st16c(ones, sws, vs[0]); st16c(ones, sws, vs[1]); st16c(ones, sws, vs[2]);
    }
#define VP1(c) (vp1 + (((c) + r1 + ((lane >> 2) & 1)) & 7) * (F_ * 2))
    touch(pf[0], sp1, VP1(2)); touch(pf[1], sp1, VP1(4));
    vp1 = VP1(6);
    wait_vm<20>();
    pin(xv);
    float v[14], mean, var;
#pragma unroll
    for (int j = 0; j < 7; ++j) { v[2 * j] = bflo(xv[j]); v[2 * j + 1] = bfhi(xv[j]); }
    row_stats(v, lane, mean, var);
    const float rs = ln_rstd(var), nb = -bfr(mean) * rs;
#pragma unroll
    for (int j = 0; j < 14; ++j) v[j] = fmaf(v[j], rs, nb);
    u4 xa[2];
#pragma unroll
    for (int t = 0; t < 2; ++t) xa[t] = u4{pk_h(v[t], v[2 + t]), pk_h(v[4 + t], v[6 + t]), pk_h(v[8 + t], v[10 + t]), pk_h(v[12 + t], 0.f)};
    pin(xa);
    wait_vm<9>();
    pin(tb1);
    asm volatile("" : "+v"(tag), "+v"(wa), "+v"(wb));
    *(u4*)(G1 + 16 * tid) = wa; *(u4*)(G1 + 4096 + 16 * tid) = wb;
    f4 tt4[4], xo[4];
    mfma_2x2(tt4, xa[0], xa[1], tb1[0], tb1[1]);
    const f4 pr0 = tt4[0] * tc[0] + tt4[1] * ts[0], pi0 = tt4[1] * tc[0] - tt4[0] * ts[0];
    const f4 pr1 = tt4[2] * tc[1] + tt4[3] * ts[1], pi1 = tt4[3] * tc[1] - tt4[2] * ts[1];
    mfma_pq(xo, zip4h(pr0, pr1), zip4h(pi0, pi1), tb1[2], tb1[3], tb1[4], tb1[2], tb1[5], tb1[6], tb1[6], tb1[7]);
#pragma unroll
    for (int r = 0; r < 4; ++r) *(unsigned*)(G0 + lo0 + 224 * r) = pk_h(xo[0][r], xo[1][r]);
#pragma unroll
    for (int r = 0; r < 4; ++r) *(unsigned*)(G0 + lo1 + 224 * (3 - r)) = pk_h(xo[2][r], xo[3][r]);
    lds_barrier();
    {
      u4 a[3], c[3];
#pragma unroll
      for (int i = 0; i < 3; ++i) a[i] = *(const u4*)(G0 + ls + i * 1536);
      st16c(a[0], sws, vo); st16c<3072>(a[1], sws, vo); st16c(a[2], sws, vo2);
#pragma unroll
      for (int i = 0; i < 3; ++i) {
#pragma unroll
        for (int r = 0; r < 4; ++r) {
          const unsigned short* e = (const unsigned short*)(G0 + lg + i * 192 + r * 450);
          c[i][r] = (unsigned)e[0] | ((unsigned)e[2048] << 16);
        }
      }
      st16c(c[0], sws, vo, 7296); st16c<3072>(c[1], sws, vo, 7296); st16c(c[2], sws, vo2, 7296);
    }
    SB();
    tag[0] = __builtin_amdgcn_readfirstlane(tag[0]) + 1u;
    tag[1] = __builtin_amdgcn_readfirstlane(tag[1]);
#pragma unroll
    for (int k = 0; k < 4; ++k) {
      const unsigned swz = ((lane & 7) ^ ((g & 1) << 1) ^ ((k & 1) << 2)) << 4;
      vb1[k] = (32 * (lane >> 3) + 256 * k) * (F_ * 2) + nt1 * 128 + swz - k * 1024;
      vb2[k] = ((lane >> 3) + 8 * k) * (S_ * 2) + nt2 * 128 + swz - k * 1024;
    }
#pragma unroll
    for (int i = 0; i < 2; ++i) {
      const int qq = 64 * w + 32 * i + (lane >> 1), row = qq >> 3;
      la[i] = GEN + 16384 + row * 256 + (((2 * (qq & 7) + (lane & 1)) ^ (row & 15)) << 4);
    }
    vfl = tid < NREP ? OFF_FLG + tid * FREP : OOB;
    {
      const int pr = rk * 16 + (lane >> 2);
#define RW2(r) ((r) + 32 * r2 < 448 ? (r) + 32 * r2 : (r) + 32 * r2 - 448)
      vp2 = ((pr & 3) * 448 + RW2(pr >> 2)) * (S_ * 2) + xc * 512 + (lane & 3) * 128;
    }
    asm volatile("" : "+v"(vfl), "+v"(tag), "+v"(vp2), "+v"(vp1));
    pin(la); pin(vb1); pin(vb2);
    static_for<0, 8>([&](auto I) {
      constexpr int i = I.v;
      const float a = (float)((16 * (i >> 2) + 4 * g + (i & 3)) * (16 * w + n)) * (1.0f / 2048.0f);
      const float bb = (float)((16 * (i >> 2) + n) * (16 * w + 4 * g + (i & 3))) * (1.0f / 2048.0f);
      float r[4] = {16.0f * __builtin_amdgcn_cosf(a), 16.0f * __builtin_amdgcn_sinf(a), __builtin_amdgcn_cosf(bb), __builtin_amdgcn_sinf(-bb)};
      pin(r);
      c1[i >> 2][i & 3] = r[0]; s1[i >> 2][i & 3] = r[1]; c2[i >> 2][i & 3] = r[2]; s2[i >> 2][i & 3] = r[3];
      DP1(4 + (i >> 2), i >> 2, i & 3);
    });
    if (w < 3) wait_vm<8>();
    lds_barrier();
    st8c(tag, sws, vfl, FL1 + q * FS);
  }

  {
#pragma unroll
    for (int i = 0; i < 4; ++i) ld16(bc[i], stab, lane * 16, (w * 4 + i) * 1024);
    {
      const unsigned vt[4] = {(unsigned)lane * 16, (unsigned)lane * 16, (unsigned)lane * 16, (unsigned)lane * 16};
      dma2<0>(stab, vt, 16384 + w * 4096, lw - w * WREG + GEN + 8192 + (w >> 1) * 16384 + (w & 1) * 4096);
    }
    touch(pg[0], sp1, vp1);
    touch(pg[1], sp2, vp2);
    SB();
    vc = OFF_XW + col * 32 + (w * 64 + (lane >> 1)) * PXW + (lane & 1) * 16;
    vf = fbase + FL1 + tid * FS;
    lwv = OFF_XW + 7296 + col * 32 + (32 * g + 2 * w + (n >> 3)) * PXW + (n & 7) * 4; lu = GEN + n * 256 + (((4 * w + g) ^ n) << 4);
    vy = OFF_Y + col * PY + n * 256 + (4 * w + g) * 16;
    asm volatile("" : "+v"(vc), "+v"(vf), "+v"(lwv), "+v"(lu), "+v"(vy));
    {
      const int q4 = n >> 2;
#pragma unroll
      for (int c = 0; c < 4; ++c) rbo[c] = w * WREG + (8 * g + q4) * 128 + (lane & 3) * 8 + ((c ^ w ^ (q4 >> 1) ^ ((g & 1) << 1)) << 5);
#pragma unroll
      for (int ks = 0; ks < 4; ++ks) lr[ks] = GEN + n * 256 + (((4 * ks + g) ^ n) << 4);
      pin(rbo); pin(lr);
      const int pr = rk * 16 + (lane >> 2);
      vp3 = ((pr & 3) * 448 + RW2(128 + (pr >> 2))) * (S_ * 2) + xc * 512 + (lane & 3) * 128;
      asm volatile("" : "+v"(vp3));
    }
    static_for<0, 8>([&](auto I) {
      constexpr int i = I.v;
      DP1(6 + (i >> 2), 2 + (i >> 2), i & 3);
      const float cc = (float)((2 * n + (i >> 2)) * (4 * (g & 1) + (i & 3))) * (1.0f / 448.0f);
      float r[2] = {__builtin_amdgcn_cosf(cc), __builtin_amdgcn_sinf(cc)};
      const int kf = (n & 7) + 14 * (4 * g + (i & 3));
      const int k2 = (i < 4) ? kf : 224 - kf;
      int e[1] = {GEN + 8192 + k2 * 32 + ((wn ^ ((k2 >> 2) & 1)) << 4) + (2 * (w & 1) + (n >> 3)) * 4};
      pin(r); pin(e);
      tc3[i >> 2][i & 3] = r[0]; ts3[i >> 2][i & 3] = r[1]; ad[i] = e[0];
    });
    if (rk > 28) {
      const u4 t1 = make_srd(w1, S_ * F_ * 2);
      unsigned d[3];
#pragma unroll
      for (int i = 0; i < 3; ++i) {
        const int r = (i * 12 + (rk - 29) * 4 + w) * 16 + (lane >> 2);
        touch(d[i], t1, ((r >> 1) * 8 + (((r & 1) + r1) & 7)) * (F_ * 2) + ((7 * xc) >> 1) * 128 + (lane & 3) * 128);
      }
      wait_vm<0>();
      pin(d);
    }
    poll_flag(sws, vf, tag);
    pin(pf); pin(pg); pin(bc);
    asm volatile("" : "+v"(ev), "+v"(braw), "+v"(pft), "+v"(pfc));
  }
  const Dma d1 = {sw1, {vb1[0], vb1[1], vb1[2], vb1[3]}, lw};
#define TRF(B, e, s) B[e] = trfrag(lds + rbo[(e) & 3] + (s) * 4096)
#define TRP(B, p, s) do { TRF(B, 4 * (p), s); TRF(B, 4 * (p) + 1, s); TRF(B, 4 * (p) + 2, s); TRF(B, 4 * (p) + 3, s); } while (0)
  u4 B1[64], B2[56];

  if (b >= DF_) {
    asm volatile("s_barrier" ::: "memory");
#pragma unroll
    for (int i = 0; i < 10; ++i) tb3[i] = *(const u4*)(lds + GEN + lane * 16 + (i < 2 ? 8192 + (6 + i) * 1024 : 24576 + (i - 2) * 1024));
    lds_barrier();
  } else {
    u4 xw[2];
    unsigned wv[8];
    ld16c(xw[0], sws, vc, 0); ld16c(xw[1], sws, vc, 32 * PXW);
    SB();
    u4 bs[4], fb[6];
#pragma unroll
    for (int i = 0; i < 4; ++i) {
#pragma unroll
      for (int j = 0; j < 4; ++j) bs[i][j] = __builtin_amdgcn_alignbit(bc[i][j], bc[i][j], 16) ^ 0x8000u;
    }
    pin(bs);
    asm volatile("s_barrier" ::: "memory");
#pragma unroll
    for (int i = 0; i < 8; ++i)
      asm volatile("buffer_load_dword %0, %2, %1, %3 offen sc1" : "=v"(wv[i]) : "s"(sws), "v"(lwv), "s"((128 * (i >> 2) + 8 * (i & 3)) * PXW) : "memory");
    wait_vm<8>();
    pin(xw);
#pragma unroll
    for (int i = 0; i < 2; ++i) *(u4*)(lds + la[i]) = xw[i];
    lds_barrier();
    u4 dd[8];
#pragma unroll
    for (int i = 0; i < 8; ++i) dd[i] = *(const u4*)(lds + lr[i & 3] + 16384 + (i >> 2) * 4096);
    asm volatile("" ::: "memory");
#pragma unroll
    for (int i = 0; i < 6; ++i) fb[i] = *(const u4*)(lds + GEN + 8192 + lane * 16 + i * 1024);
    f4 o[4];
    mfma_dft64_d<false, 4>(o, dd, bc, bs, d1, SO1(8));
#pragma unroll
    for (int i = 0; i < 10; ++i) tb3[i] = *(const u4*)(lds + GEN + lane * 16 + (i < 2 ? 8192 + (6 + i) * 1024 : 24576 + (i - 2) * 1024));
    {
      const u4 dr = pk4h(o[0] * c1[0] + o[1] * s1[0], o[2] * c1[1] + o[3] * s1[1]);
      const u4 di = pk4h(o[1] * c1[0] - o[0] * s1[0], o[3] * c1[1] - o[2] * s1[1]);
      mfma_pq_d<true, 5>(o, dr, di, fb[0], fb[1], fb[2], fb[0], fb[3], fb[4], fb[5], fb[3], d1, SO1(9));
    }
    wait_vm<8>();
    pin(wv);
    f4 wr_[2], wi_[2];
#pragma unroll
    for (int i = 0; i < 8; ++i) { wr_[i >> 2][i & 3] = bflo(wv[i]); wi_[i >> 2][i & 3] = bfhi(wv[i]); }
    {
      const u4 dr = pk4h(o[0] * wr_[0] - o[1] * wi_[0], o[2] * wr_[1] - o[3] * wi_[1]);
      const u4 di = pk4h(o[0] * wi_[0] + o[1] * wr_[0], o[2] * wi_[1] + o[3] * wr_[1]);
      mfma_pq_d<false, 6>(o, dr, di, fb[0], fb[2], fb[1], fb[0], fb[3], fb[5], fb[4], fb[3], d1, SO1(10));
    }
#pragma unroll
    for (int t = 0; t < 2; ++t)
      *(u4*)(lds + lu + t * 4096) = zip4h(o[2 * t] * c2[t] + o[2 * t + 1] * s2[t], o[2 * t] * s2[t] - o[2 * t + 1] * c2[t]);
    lds_barrier();
#pragma unroll
    for (int i = 0; i < 8; ++i) dd[i] = *(const u4*)(lds + lr[i & 3] + (i >> 2) * 4096);
    mfma_dft64_d<true, 7>(o, dd, bc, bs, d1, SO1(11));
#pragma unroll
    for (int t = 0; t < 2; ++t) {
      u4 y = zip4h(o[2 * t], o[2 * t + 1]);
#pragma unroll
      for (int r = 0; r < 4; ++r) y[r] = y[r] < 0xfffffffeu ? y[r] : 0xfffffffeu;
      st16c(y, sws, vy, 4096 * t);
    }
    st8c(tag, sws, (b == 0 && tid == 0) ? OFF_EPOCH : OOB, 0);
    wait_vm<15>();
    TRP(B1, 4, 0); asm volatile("" ::: "memory"); SB(); TRP(B1, 5, 1); wait_lgkm8(); DMA1(12, 0);
    TRP(B1, 6, 2); wait_lgkm8(); DMA1(13, 1);
    TRP(B1, 7, 3); wait_lgkm8(); DMA1(14, 2);
    TRP(B1, 8, 4); wait_lgkm8(); DMA1(15, 3);
    wait_lgkm(); DMA1(0, 4);
  }

  {
    int lh = GEN + 24576 + w * 2048 + (g * 112 + n) * 4, lh2 = GEN + 24576 + w * 2048 + lane * 16;
    int lp6 = GEN + 16384 + (w * 448 + (g & 1) * 224 + 2 * n) * 4, lp = lp6 + hh * 128, lq = GEN + 16384 + tid * 4;
    unsigned vn0 = OFF_HN + (q * 448 + tid) * 16, vn1 = tid < 192 ? vn0 + 4096 : OOB;
    unsigned vh0 = OFF_H + lane * 16 + (lane < 56 ? 0 : 31 * D_ * 2), vh1 = lane < 48 ? OFF_H + 1024 + lane * 16 + 31 * D_ * 2 : OOB;
    {
      const int i1 = tid < 194 ? tid + 256 : tid;
      const unsigned v0 = OFF_Y + (tid >> 1) * PY + q * 32 + (tid & 1) * 16;
      const unsigned v1 = OFF_Y + (i1 >> 1) * PY + q * 32 + (i1 & 1) * 16;
      int l0 = (tid >> 1) * 32 + (((tid & 1) ^ ((tid >> 3) & 1)) << 4), l1 = (i1 >> 1) * 32 + (((i1 & 1) ^ ((i1 >> 3) & 1)) << 4);
      u4 y0, y1;
      if (b >= DF_) {
        const unsigned vs = lane == 0 ? OFF_Y + ((b - DF_) * 7 + w) * PY + q * 32 : OOB;
#pragma unroll 1
        for (unsigned sp = 0; sp < SPIN_CAP; ++sp) {
          unsigned f;
          asm volatile("buffer_load_dword %0, %2, %1, 0 offen sc1\n\ts_waitcnt vmcnt(0)" : "=v"(f) : "s"(sws), "v"(vs) : "memory");
          if (__builtin_amdgcn_ballot_w64(f == 0xffffffffu) == 0ull) break;
          __builtin_amdgcn_s_sleep(8);
        }
        __builtin_amdgcn_s_sleep(4);
      }
      ld16c(y0, sws, v0, 0); ld16c(y1, sws, v1, 0);
      asm volatile("" : "+v"(lh), "+v"(lh2), "+v"(lp), "+v"(lp6), "+v"(lq), "+v"(vn0), "+v"(vn1), "+v"(vh0), "+v"(vh1));
      wait_vm<22>();
      TRP(B1, 9, 5); TRP(B1, 10, 6); TRP(B1, 11, 7);
      wait_vm<10>();
      TRP(B1, 12, 0); TRP(B1, 13, 1); TRP(B1, 14, 2);
      asm volatile("s_waitcnt vmcnt(0)" : "+v"(y0), "+v"(y1), "+v"(l0), "+v"(l1)::"memory");
#pragma unroll 1
      for (unsigned sp = 0; sp < SPIN_CAP; ++sp) {
        const bool bad0 = max(max(y0[0], y0[1]), max(y0[2], y0[3])) == 0xffffffffu;
        const bool bad1 = max(max(y1[0], y1[1]), max(y1[2], y1[3])) == 0xffffffffu;
        if (__builtin_amdgcn_ballot_w64(bad0 || bad1) == 0ull) break;
        __builtin_amdgcn_s_sleep(2);
        u4 n0, n1;
        asm volatile("buffer_load_dwordx4 %0, %3, %2, 0 offen sc1\n\tbuffer_load_dwordx4 %1, %4, %2, 0 offen sc1\n\ts_waitcnt vmcnt(0)"
                     : "=&v"(n0), "=&v"(n1) : "s"(sws), "v"(bad0 ? v0 : OOB), "v"(bad1 ? v1 : OOB) : "memory");
        if (bad0) y0 = n0;
        if (bad1) y1 = n1;
      }
      *(u4*)(G1 + l0) = y0;
      *(u4*)(G1 + l1) = y1;
    }
    lds_barrier();
    unsigned pt;
    touch(pt, sp2, vp3);
    u4 a0, a1;
#pragma unroll
    for (int i = 0; i < 4; ++i) { a0[i] = *(const unsigned*)(lds + ad[i]); a1[i] = *(const unsigned*)(lds + ad[4 + i]); }
    f4 o[4], y[4];
    mfma_pq_d<false, 0>(o, a0, a1, tb3[0], tb3[1], tb3[2], tb3[3], tb3[4], tb3[5], tb3[6], tb3[7], d1, SO1(1));
    {
      const u4 b0 = pk4h(o[0] * tc3[0] - o[1] * ts3[0], o[0] * ts3[0] + o[1] * tc3[0]);
      const u4 b1v = pk4h(o[2] * tc3[1] - o[3] * ts3[1], o[2] * ts3[1] + o[3] * tc3[1]);
      mfma_2x2_d<1>(y, tb3[8], tb3[9], b0, b1v, d1, SO1(2));
    }
    float v[14], mean, var;
#pragma unroll
    for (int j = 0; j < 7; ++j) {
      const unsigned hp = pk_bf(fmaf(y[2 * (j >> 2)][j & 3], 1.0f / 224.0f, bflo(xv[j])), fmaf(y[2 * (j >> 2) + 1][j & 3], 1.0f / 224.0f, bfhi(xv[j])));
      *(unsigned*)(lds + lh + 64 * j) = hp;
      v[2 * j] = bflo(hp); v[2 * j + 1] = bfhi(hp);
    }
    DMA1(3, 2);
    row_stats(v, lane, mean, var);
    const float mb = bfr(mean), rs = ln_rstd(var);
    const float m2 = bfr((mean - mb) * rs);
    const float rs2 = ln_rstd(var * rs * rs);
    const float al = rs * rs2, be = -m2 * rs2 - mb * al;
    unsigned pk[8];
#pragma unroll
    for (int j = 0; j < 7; ++j) pk[j] = pk_bf(fmaf(v[2 * j], al, be), fmaf(v[2 * j + 1], al, be));
    pk[7] = pk[6];
#pragma unroll
    for (int j = 0; j < 8; j += 2) {
      const auto r = __builtin_amdgcn_permlane32_swap(pk[j], pk[j + 1], false, false);
      const u2 e = {__builtin_amdgcn_perm(r[1], r[0], 0x05040100u), __builtin_amdgcn_perm(r[1], r[0], 0x07060302u)};
      *(u2*)(lds + (j < 6 ? lp : lp6) + 128 * j) = e;
    }
    lds_barrier();
    {
      const unsigned* pl = (const unsigned*)(lds + lq);
      const u4 o0 = {pl[0], pl[448], pl[896], pl[1344]};
      const u4 o1 = {pl[256], pl[448 + 256], pl[896 + 256], pl[1344 + 256]};
      const u4 h0 = *(const u4*)(lds + lh2), h1 = *(const u4*)(lds + lh2 + 1024);
      st16c(o0, sws, vn0); st16c(o1, sws, vn1);
      st16c(h0, sws, vh0, sxo); st16c(h1, sws, vh1, sxo);
    }
    DMA2(0, 5); DMA2(1, 6);
    TRP(B1, 15, 3); TRP(B1, 0, 4);
    wait_vm<8>(pt);
    lds_barrier();
    st8c(tag, sws, vfl, q * FS);
    TRP(B1, 1, 0); TRP(B1, 2, 1); TRP(B1, 3, 2);
    DMA2(2, 3); DMA2(3, 4);
    wait_lgkm();
    DMA2(4, 0); DMA2(5, 1); DMA2(6, 2);
  }
  if (!has2) {
    const u4 sp = make_srd(w2, F_ * S_ * 2);
    const int r = ((rk - 28) * 4 + w) * 16 + (lane >> 2);
    unsigned d[3];
#pragma unroll
    for (int i = 0; i < 3; ++i) touch(d[i], sp, ((r & 3) * 448 + RW2(256 + 64 * i + (r >> 2))) * (S_ * 2) + xc * 512 + (lane & 3) * 128);
    wait_vm<0>();
    pin(d);
    return;
  }
  unsigned va = (g * D_ + n) * 16;
  unsigned eo = ((nt2 * 64 + w * 16 + (lane >> 3)) * D_ + dt2 * 64) * 2 + (lane & 7) * 16;
  unsigned vg = lane < 28 ? fbase + FLUA + (dt2 * 28 + 7 * w + (lane >> 2)) * 128 + (lane & 3) * 8 : OOB;
  unsigned fu = lane < NREP ? OFF_FLG + lane * FREP + (dt1 * 28 + nt1) * 128 + w * 8 : OOB;
  asm volatile("" : "+v"(va), "+v"(eo), "+v"(vg), "+v"(fu));
  u4 fa[GL];
  poll_flag(sws, fbase + tid * FS, tag);
#define LDA(G, H, u) ld16c<(2 * ((u) >= (H)) + ((u) & 1)) * 256>(fa[(u) % GL], sa, va,                                      \
                                                                 (G == 1 ? ROT1(((u) % (H)) >> 1) : ROT2(((u) % (H)) >> 1)) * SA)
#define MF(G, B, H, u, c) do { constexpr int i_ = 2 * (c) + ((u) & 1), e_ = 4 * (((u) % (H)) >> 1) + (c);                  \
    if constexpr (G == 1) ac[i_] = MFMA16B(B[e_], fa[(u) % GL], ac[i_]);                                                    \
    else ac[i_] = MFMA16B(fa[(u) % GL], B[e_], ac[i_]);                                                                     \
    asm volatile("" : "+a"(ac[i_])); SB(); } while (0)
#define RED_W(buf, A, c) do { *(f4*)(lds + red_off(buf, w ^ (c), c) + lane * 16) = A[2 * (c)];                              \
                              *(f4*)(lds + red_off(buf, w ^ (c), c) + 1024 + lane * 16) = A[2 * (c) + 1]; } while (0)
#define RED_R(buf) _Pragma("unroll") for (int i = 0; i < 6; ++i)                                                            \
                     rp[i] = *(const f4*)(lds + red_off(buf, w, 1 + (i >> 1)) + (i & 1) * 1024 + lane * 16)
#define RED_S(A, t) ((A[t] + rp[t]) + (rp[2 + (t)] + rp[4 + (t)]))

  if (has1) {
    const u4 sa = make_srd(ws + OFF_HN + w * (16 * SA) + dt1 * 1024, 16 * SA);
    static_for<0, GL>([&](auto U) { LDA(1, 32, U.v); });
    SB();
    unsigned vo = OFF_UT + ((nt1 * 8 + w * 2 + (g >> 1)) * D_ + dt1 * 64 + 16 * (g & 1) + n) * 16;
    asm volatile("" : "+v"(vo));
    f4 acca[8] = {}, accb[8] = {}, rp[6], sm[2];
    unsigned o[4];
    auto put = [&](auto P) {
      const auto r0 = __builtin_amdgcn_permlane16_swap(o[0], o[2], false, false);
      const auto r1 = __builtin_amdgcn_permlane16_swap(o[1], o[3], false, false);
      const u4 ov = {r0[0], r1[0], r0[1], r1[1]};
      st16c<512 * P.v>(ov, sws, vo);
    };
    wait_vm<GL - 1>(fa[0]);
    static_for<0, S1::N>([&](auto U) {
      constexpr int u = U.v, ul = u - 1 + GL;
      f4 (&ac)[8] = u < 32 ? acca : accb;
      MF(1, B1, 32, u, 0);
      if constexpr (u >= 1 && ul < S1::N) { LDA(1, 32, ul); SB(); }
      MF(1, B1, 32, u, 1);
      if constexpr (u >= 34 && u < 62) { TRF(B2, u - 34, b2_slot((u - 34) >> 2)); SB(); }
      MF(1, B1, 32, u, 2);
      if constexpr (u >= 33 && u < 36) RED_W(0, acca, u - 32);
      if constexpr (u == 37) lds_barrier();
      if constexpr (u == 38) { RED_R(0); }
      if constexpr (u == 40) { sm[0] = RED_S(acca, 0); sm[1] = RED_S(acca, 1); pin(sm); }
      if constexpr (u >= 41 && u < 45) {
        constexpr int k = u - 41;
        unsigned e[1] = {gelu_pk(sm[k >> 1][2 * (k & 1)], sm[k >> 1][2 * (k & 1) + 1], ev[k & 1])};
        pin(e);
        o[k] = e[0];
      }
      if constexpr (u == S1::TS) put(IC<0>{});
      if constexpr (u == S1::TF) { wait_vm<pend_extra<S1>(S1::TS, u, u - 1)>(); st8c(tag, sws, fu, FLUA); }
      if constexpr (u + 1 < S1::N) wait_vm<pend_load<S1>(u + 1, u)>(fa[(u + 1) % GL]);
      SB();
      MF(1, B1, 32, u, 3);
    });
    wait_lgkm();
    DMA2(7, 5);
    RED_W(1, accb, 1); RED_W(1, accb, 2); RED_W(1, accb, 3);
    DMA2(8, 6);
    lds_barrier();
    RED_R(1);
    DMA2(9, 3);
    sm[0] = RED_S(accb, 0); sm[1] = RED_S(accb, 1);
    static_for<0, 4>([&](auto K) {
      constexpr int k = K.v;
      unsigned e[1] = {gelu_pk(sm[k >> 1][2 * (k & 1)], sm[k >> 1][2 * (k & 1) + 1], ev[k & 1])};
      pin(e);
      o[k] = e[0];
      DP2(10, 4, k);
    });
    put(IC<1>{});
    DMA2(11, 0); DMA2(12, 1); DMA2(13, 2);
    u2 f;
    ld8c<0>(f, sws, vg);
    wait_vm<13>();
    st8c(tag, sws, fu, FLUB);
    wait_vm<1>(f);
    if (f[0] == tag[0] && f[1] == tag[1]) vg |= OOB;
  } else {
    TRP(B2, 0, 5); TRP(B2, 1, 6); TRP(B2, 2, 3); TRP(B2, 3, 4); TRP(B2, 4, 0); TRP(B2, 5, 1); TRP(B2, 6, 2);
    lds_barrier();
    __builtin_amdgcn_s_sleep(80);
    static_for<7, 14>([&](auto P) { DMA2(P.v, b2_slot(P.v)); });
    wait_vm<0>();
  }

  {
    const u4 sa = make_srd(ws + OFF_UT + w * (14 * SA) + dt2 * 1024, 14 * SA);
    if (__builtin_amdgcn_ballot_w64(vg < OOB) != 0ull) poll_flag(sws, vg < OOB ? vg : OOB, tag);
    static_for<0, GL>([&](auto U) { LDA(2, 28, U.v); });
    SB();
    static_for<7, 14>([&](auto P) { TRP(B2, P.v, b2_slot(P.v)); });
    vg = lane < 28 ? (vg & ~OOB) + (FLUB - FLUA) : OOB;
    const float bv = (n & 1) ? bfhi(braw) : bflo(braw);
    int lo = w * WREG + 5 * 4096 + n * 144 + g * 8;
    asm volatile("" : "+v"(vg), "+v"(lo));
    f4 acca[8] = {}, accb[8] = {}, rp[6];
    u4 hv[2];
    u2 f;
    auto put = [&](const f4& sm, int t) {
      const u2 o = {pk_bf(bfr(sm[0]) + bv, bfr(sm[1]) + bv), pk_bf(bfr(sm[2]) + bv, bfr(sm[3]) + bv)};
      *(u2*)(lds + lo + t * 32) = o;
    };
    wait_vm<GL - 1>(fa[0]);
    static_for<0, S2::N>([&](auto U) {
      constexpr int u = U.v;
      f4 (&ac)[8] = u < 28 ? acca : accb;
      MF(2, B2, 28, u, 0);
      if constexpr (u == S2::TG) {
        wait_vm<pend_extra<S2>(S2::TP, u - 1, u - 1)>(f);
        if (f[0] == tag[0] && f[1] == tag[1]) vg = OOB;
        if (__builtin_amdgcn_ballot_w64(vg != OOB) != 0ull) poll_flag(sws, vg, tag);
      }
      static_for<GL, S2::N>([&](auto V) { if constexpr (S2::issue(V.v) == u) LDA(2, 28, V.v); });
      SB();
      MF(2, B2, 28, u, 1);
      MF(2, B2, 28, u, 2);
      if constexpr (u == S2::TP) ld8c<0>(f, sws, vg);
      if constexpr (u == S2::TH) { ld16c(hv[0], sws, eo, OFF_H); ld16c(hv[1], sws, eo, OFF_H + 8 * D_ * 2); }
      if constexpr (u >= 29 && u < 32) RED_W(0, acca, u - 28);
      if constexpr (u == 34) lds_barrier();
      if constexpr (u == 35) { RED_R(0); }
      if constexpr (u == 37 || u == 38) put(RED_S(acca, u - 37), u - 37);
      if constexpr (u + 1 < S2::N) wait_vm<pend_load<S2>(u + 1, u)>(fa[(u + 1) % GL]);
      SB();
      MF(2, B2, 28, u, 3);
    });
    pin(hv);
    RED_W(1, accb, 1); RED_W(1, accb, 2); RED_W(1, accb, 3);
    lds_barrier();
    RED_R(1);
    put(RED_S(accb, 0), 2); put(RED_S(accb, 1), 3);
    const u4 sout = make_srd(out, S_ * D_ * 2);
#pragma unroll
    for (int k = 0; k < 2; ++k) {
      const u4 yv = *(const u4*)(lds + w * WREG + 5 * 4096 + (8 * k + (lane >> 3)) * 144 + (lane & 7) * 16);
      u4 o;
#pragma unroll
      for (int j = 0; j < 4; ++j) o[j] = pk_bf(bflo(yv[j]) + bflo(hv[k][j]), bfhi(yv[j]) + bfhi(hv[k][j]));
      asm volatile("buffer_store_dwordx4 %0, %2, %1, %3 offen\n\ts_nop 1" ::"v"(o), "s"(sout), "v"(eo), "s"(k * (8 * D_ * 2)) : "memory");
    }
  }
}

extern "C" void kernel_launch(void* const* d_in, const int* in_sizes, int n_in, void* d_out, int out_size, void* d_ws,
                              size_t ws_size, hipStream_t stream) {
  typedef const unsigned char* cp;
  k_mega<<<256, 256, 0, stream>>>((unsigned char*)d_ws, (cp)d_in[0], (cp)d_in[1], (cp)d_in[2], (cp)d_in[4], (cp)d_in[5],
                                  (cp)d_in[6], (cp)d_in[7], (unsigned char*)d_out);
}
```
